```python
import torch

def _softmax(x, axis=-1):
    return torch.softmax(x, dim=axis)
def _gelu(x):
    return torch.nn.functional.gelu(x, approximate="tanh")
def _silu(x):
    return torch.nn.functional.silu(x)
def _sigmoid(x):
    return torch.sigmoid(x)
def _layernorm(x, axis=-1, eps=1e-5):
    m = x.mean(dim=axis, keepdim=True)
    v = x.var(dim=axis, keepdim=True, unbiased=False)
    return (x - m) / torch.sqrt(v + eps)
def _scatter_add(tgt, idx, src, axis=0):
    return tgt.clone().index_add_(axis, idx, src)
def _segment_sum(src, seg, n):
    out = torch.zeros((n,) + src.shape[1:], dtype=src.dtype)
    return out.index_add_(0, seg, src)
def _pad(x, pw):
    flat = tuple(v for pair in reversed(pw) for v in pair)
    return torch.nn.functional.pad(x, flat)

kernel_name = 'parallel_sum__sliding_block_cached__glu_gated_glu__decode'

def setup_inputs():

    _w = (lambda a: (lambda t: t.to(torch.bfloat16) if t.is_floating_point() else t)(torch.as_tensor(a)))
    def _A_setup():
        B, Sq, D = 2, 1, 512
        Hq, Hkv, Sk, Dh = 32, 32, 4096, 128
        assert Hq % Hkv == 0, (Hq, Hkv)
        return {
            "x": _w((lambda *s: torch.randn(*s, dtype=torch.bfloat16) * 0.05)(B, Sq, D)),
            "k_cache": _w((lambda *s: torch.randn(*s, dtype=torch.bfloat16) * 3.2)(B, Hkv, Sk, Dh)),
            "v_cache": _w((lambda *s: torch.randn(*s, dtype=torch.bfloat16) * 1.6)(B, Hkv, Sk, Dh)),
            "wq": _w((lambda *s: torch.randn(*s, dtype=torch.bfloat16) * 3.2)(D, Hq * Dh) * (D ** -0.5)),
            "wo": _w((lambda *s: torch.randn(*s, dtype=torch.bfloat16) * 1.6)(Hq * Dh, D) * ((Hq * Dh) ** -0.5)),
        }
    _A_in = _A_setup()

    def _B_setup():
        B, S, D, F = 2, 1, 512, 1376
        return {
            "x": _w((lambda *s: torch.randn(*s, dtype=torch.bfloat16) * 0.05)(B, S, D)),
            "w_gate": _w((lambda *s: torch.randn(*s, dtype=torch.bfloat16) * 0.2)(D, F)),
            "w_up": _w((lambda *s: torch.randn(*s, dtype=torch.bfloat16) * 0.05)(D, F)),
            "w_down": _w((lambda *s: torch.randn(*s, dtype=torch.bfloat16) * 0.05)(F, D)),
        }
    _B_in = _B_setup()

    return {
        "A_x": _A_in["x"],
        "A_k_cache": _A_in["k_cache"],
        "A_v_cache": _A_in["v_cache"],
        "A_wq": _A_in["wq"],
        "A_wo": _A_in["wo"],
        "B_x": _B_in["x"],
        "B_w_gate": _B_in["w_gate"],
        "B_w_up": _B_in["w_up"],
        "B_w_down": _B_in["w_down"],
    }

def reference(A_x, A_k_cache, A_v_cache, A_wq, A_wo, B_x, B_w_gate, B_w_up, B_w_down):
    def _A_ref(x, k_cache, v_cache, wq, wo):
        B, Sq, D = x.shape
        Hq, Dh = 32, 128
        Hkv, Sk = k_cache.shape[1], k_cache.shape[2]
        W = min(4096, max(256, Sk // 4))
        rep = Hq // Hkv
        q = (lambda x, *d: x.permute(*d))((x @ wq).reshape(B, Sq, Hq, Dh), 0, 2, 1, 3)
        k = (lambda x, n, axis: torch.repeat_interleave(x, n, dim=axis))(k_cache[:, :, Sk - W:, :], rep, axis=1)
        v = (lambda x, n, axis: torch.repeat_interleave(x, n, dim=axis))(v_cache[:, :, Sk - W:, :], rep, axis=1)
        scores = torch.einsum("bhqd,bhkd->bhqk", q, k) * (Dh ** -0.5)
        p = _softmax(scores, axis=-1)
        o = torch.einsum("bhqk,bhkd->bhqd", p, v)
        return (lambda x, *d: x.permute(*d))(o, 0, 2, 1, 3).reshape(B, Sq, Hq * Dh) @ wo

    def _B_ref(x, w_gate, w_up, w_down):
        g = torch.einsum("bsd,df->bsf", x, w_gate)
        u = torch.einsum("bsd,df->bsf", x, w_up)
        h = _sigmoid(g) * u
        return torch.einsum("bsf,fd->bsd", h, w_down)

    x = A_x
    xn = _layernorm(x, axis=-1)
    return x + _A_ref(xn, A_k_cache, A_v_cache, A_wq, A_wo) + _B_ref(xn, B_w_gate, B_w_up, B_w_down)
```

```cpp
#include <hip/hip_runtime.h>
#include <hip/hip_bf16.h>
#include <stdint.h>

#define MIMIC 1

typedef unsigned short u16;
__device__ __forceinline__ float bf2f(u16 v) { return __uint_as_float(((unsigned)v) << 16); }
__device__ __forceinline__ float rbf(float x) {
  unsigned u = __float_as_uint(x);
  u = (u + 0x7FFFu + ((u >> 16) & 1u)) & 0xFFFF0000u;
  return __uint_as_float(u);
}
__device__ __forceinline__ float mr(float x) { return MIMIC ? rbf(x) : x; }

constexpr int B = 2, D = 512, H = 32, SK = 4096, DH = 128, W = 1024, F = 1376;

__global__ void k_ln(const u16* x, float* xn) {
  int b = blockIdx.x; int t = threadIdx.x;
  __shared__ float red[512];
  float v = bf2f(x[b * D + t]);
  red[t] = v; __syncthreads();
  for (int s = 256; s > 0; s >>= 1) { if (t < s) red[t] += red[t + s]; __syncthreads(); }
  float mean = red[0] / D; __syncthreads();
  float d = v - mean; red[t] = d * d; __syncthreads();
  for (int s = 256; s > 0; s >>= 1) { if (t < s) red[t] += red[t + s]; __syncthreads(); }
  float var = red[0] / D;
  float m = mr(mean), vr = mr(var);
  float num = mr(v - m);
  float den = mr(sqrtf(mr(vr + 1e-5f)));
  xn[b * D + t] = mr(num / den);
}

__global__ void k_q(const float* xn, const u16* wq, float* q) {
  int j = blockIdx.x * blockDim.x + threadIdx.x;
  float a0 = 0, a1 = 0;
  for (int d = 0; d < D; ++d) { float w = bf2f(wq[(size_t)d * 4096 + j]); a0 += xn[d] * w; a1 += xn[D + d] * w; }
  q[j] = mr(a0); q[4096 + j] = mr(a1);
}

__global__ void k_h(const float* xn, const u16* wg, const u16* wu, float* h) {
  int j = blockIdx.x * blockDim.x + threadIdx.x; if (j >= F) return;
  float g0 = 0, g1 = 0, u0 = 0, u1 = 0;
  for (int d = 0; d < D; ++d) { float a = bf2f(wg[(size_t)d * F + j]), c = bf2f(wu[(size_t)d * F + j]);
    g0 += xn[d] * a; g1 += xn[D + d] * a; u0 += xn[d] * c; u1 += xn[D + d] * c; }
  g0 = mr(g0); g1 = mr(g1); u0 = mr(u0); u1 = mr(u1);
  float s0 = mr(1.f / (1.f + expf(-g0))), s1 = mr(1.f / (1.f + expf(-g1)));
  h[j] = mr(s0 * u0); h[F + j] = mr(s1 * u1);
}

__global__ void k_attn(const float* q, const u16* kc, const u16* vc, float* o) {
  int bh = blockIdx.x; int t = threadIdx.x;
  __shared__ float sc[W]; __shared__ float red[256]; __shared__ float qs[DH];
  int b = bh / H, h = bh % H;
  if (t < DH) qs[t] = q[b * 4096 + h * DH + t];
  __syncthreads();
  const u16* kb = kc + ((size_t)bh * SK + (SK - W)) * DH;
  const u16* vb = vc + ((size_t)bh * SK + (SK - W)) * DH;
  float mx = -INFINITY;
  for (int k = t; k < W; k += 256) {
    float a = 0;
    for (int d = 0; d < DH; ++d) a += qs[d] * bf2f(kb[(size_t)k * DH + d]);
    a = mr(mr(a) * 0.08838834764831845f);
    sc[k] = a; mx = fmaxf(mx, a);
  }
  red[t] = mx; __syncthreads();
  for (int s = 128; s > 0; s >>= 1) { if (t < s) red[t] = fmaxf(red[t], red[t + s]); __syncthreads(); }
  mx = red[0]; __syncthreads();
  float sum = 0;
  for (int k = t; k < W; k += 256) { float e = expf(sc[k] - mx); sc[k] = e; sum += e; }
  red[t] = sum; __syncthreads();
  for (int s = 128; s > 0; s >>= 1) { if (t < s) red[t] += red[t + s]; __syncthreads(); }
  sum = red[0]; __syncthreads();
  for (int k = t; k < W; k += 256) sc[k] = mr(sc[k] / sum);
  __syncthreads();
  if (t < DH) {
    float a = 0;
    for (int k = 0; k < W; ++k) a += sc[k] * bf2f(vb[(size_t)k * DH + t]);
    o[b * 4096 + h * DH + t] = mr(a);
  }
}

__global__ void k_out(const u16* x, const float* o, const float* hh, const u16* wo, const u16* wd, u16* out) {
  int n = blockIdx.x * blockDim.x + threadIdx.x;
  int b = blockIdx.y;
  float a = 0, c = 0;
  for (int j = 0; j < 4096; ++j) a += o[b * 4096 + j] * bf2f(wo[(size_t)j * D + n]);
  for (int f = 0; f < F; ++f) c += hh[b * F + f] * bf2f(wd[(size_t)f * D + n]);
  float r = mr(bf2f(x[b * D + n]) + mr(a));
  r = rbf(r + mr(c));
  out[b * D + n] = (u16)(__float_as_uint(r) >> 16);
}

extern "C" void kernel_launch(void* const* d_in, const int* in_sizes, int n_in,
                              void* d_out, int out_size, void* d_ws, size_t ws_size,
                              hipStream_t stream) {
  const u16* x = (const u16*)d_in[0];
  const u16* kc = (const u16*)d_in[1];
  const u16* vc = (const u16*)d_in[2];
  const u16* wq = (const u16*)d_in[3];
  const u16* wo = (const u16*)d_in[4];
  const u16* wg = (const u16*)d_in[6];
  const u16* wu = (const u16*)d_in[7];
  const u16* wd = (const u16*)d_in[8];
  float* ws = (float*)d_ws;
  float* xn = ws; float* q = ws + 1024; float* h = q + 8192; float* o = h + 4096;
  k_ln<<<2, 512, 0, stream>>>(x, xn);
  k_q<<<16, 256, 0, stream>>>(xn, wq, q);
  k_h<<<(F + 255) / 256, 256, 0, stream>>>(xn, wg, wu, h);
  k_attn<<<64, 256, 0, stream>>>(q, kc, vc, o);
  k_out<<<dim3(2, 2), 256, 0, stream>>>(x, o, h, wo, wd, (u16*)d_out);
}
```

```cpp
#include <hip/hip_runtime.h>
#include <stdint.h>

typedef unsigned short u16;
typedef unsigned v4u __attribute__((ext_vector_type(4)));
typedef unsigned v2u __attribute__((ext_vector_type(2)));
typedef float f32x4 __attribute__((ext_vector_type(4)));
typedef float f32x2 __attribute__((ext_vector_type(2)));
typedef short bf16x8 __attribute__((ext_vector_type(8)));
typedef __bf16 bf16x2 __attribute__((ext_vector_type(2)));
#define LAS __attribute__((address_space(3)))

#define DELAY1 60
#define DELAYM 40
#define DELAY2 0
#define DELAY3 30
#define CPRIO 1

namespace {
constexpr int AUX_NT = 2, AUX_SC1 = 16;
constexpr unsigned RSRC3 = 0x00020000u;
constexpr int WS_EPOCH = 0;
constexpr int WS_S1Q = 4096;
constexpr int WS_S1M = WS_S1Q + 256 * 2048;
constexpr int WS_S2 = WS_S1M + 256 * 1024;
constexpr int WS_S3 = WS_S2 + 256 * 2048;
constexpr int WS_END = WS_S3 + 256 * 2048;
constexpr int WS_STAMP = 8 << 20;
constexpr int L_SYNC = 0;
constexpr int L_STAT = 64;
constexpr int L_Q = 128;
constexpr int L_X = 512;
constexpr int L_RED1 = 2560;
constexpr int L_REDM = L_RED1 + 4096;
constexpr int L_RED2 = L_REDM + 3072;
constexpr int L_OB = L_RED2 + 4096;
constexpr int L_ML = L_OB + 4096;
constexpr int L_END = L_ML + 64;
enum { C_A = 0, C_B = 1, C_M = 2, C_2 = 3, F_Q = 4, F_X = 5, F_C = 6, W_TAG = 7 };
constexpr unsigned SPIN_CAP = 1u << 22;

__device__ __forceinline__ float bf2f(unsigned v) { return __uint_as_float(v << 16); }
__device__ __forceinline__ float bflo(unsigned v) { return __uint_as_float(v << 16); }
__device__ __forceinline__ float bfhi(unsigned v) { return __uint_as_float(v & 0xffff0000u); }
__device__ __forceinline__ unsigned cvtpk(float a, float b) { f32x2 v = {a, b}; return __builtin_bit_cast(unsigned, __builtin_convertvector(v, bf16x2)); }
__device__ __forceinline__ float rbf(float a) { return bflo(cvtpk(a, a)); }
template <int CTRL> __device__ __forceinline__ unsigned dppu(unsigned x) { return (unsigned)__builtin_amdgcn_mov_dpp((int)x, CTRL, 0xf, 0xf, true); }
template <int CTRL> __device__ __forceinline__ float dppf(float x) { return __uint_as_float(dppu<CTRL>(__float_as_uint(x))); }
constexpr int XOR1 = 0xB1, XOR2 = 0x4E, XOR7 = 0x141, MIR = 0x140, ROR4 = 0x124, ROR8 = 0x128, NEWB = 0x150;
__device__ __forceinline__ float x16_sum(float x) { auto s = __builtin_amdgcn_permlane16_swap(__float_as_uint(x), __float_as_uint(x), false, false); return __uint_as_float(s[0]) + __uint_as_float(s[1]); }
__device__ __forceinline__ float x32_sum(float x) { auto s = __builtin_amdgcn_permlane32_swap(__float_as_uint(x), __float_as_uint(x), false, false); return __uint_as_float(s[0]) + __uint_as_float(s[1]); }
__device__ __forceinline__ float x16_max(float x) { auto s = __builtin_amdgcn_permlane16_swap(__float_as_uint(x), __float_as_uint(x), false, false); return fmaxf(__uint_as_float(s[0]), __uint_as_float(s[1])); }
__device__ __forceinline__ float x32_max(float x) { auto s = __builtin_amdgcn_permlane32_swap(__float_as_uint(x), __float_as_uint(x), false, false); return fmaxf(__uint_as_float(s[0]), __uint_as_float(s[1])); }
__device__ __forceinline__ f32x4 mfma(v4u a, v4u b, f32x4 c) { return __builtin_amdgcn_mfma_f32_16x16x32_bf16(__builtin_bit_cast(bf16x8, a), __builtin_bit_cast(bf16x8, b), c, 0, 0, 0); }
__device__ __forceinline__ float dot2(unsigned a, unsigned b, float c) { return __builtin_amdgcn_fdot2_f32_bf16(__builtin_bit_cast(bf16x2, a), __builtin_bit_cast(bf16x2, b), c, false); }
__device__ __forceinline__ __amdgpu_buffer_rsrc_t rsrc(const void* p, unsigned bytes) { return __builtin_amdgcn_make_buffer_rsrc((void*)p, 0, (int)bytes, RSRC3); }
__device__ __forceinline__ v4u ldnt(__amdgpu_buffer_rsrc_t r, int voff, int soff) { return __builtin_amdgcn_raw_buffer_load_b128(r, voff, soff, AUX_NT); }
__device__ __forceinline__ v4u ldc(__amdgpu_buffer_rsrc_t r, int voff) { return __builtin_amdgcn_raw_buffer_load_b128(r, voff, 0, AUX_SC1); }
__device__ __forceinline__ void stc(__amdgpu_buffer_rsrc_t r, int voff, float a, float b, unsigned tA, unsigned tB) {
  v4u v = {__float_as_uint(a), tA, __float_as_uint(b), tB};
  __builtin_amdgcn_raw_buffer_store_b128(v, r, voff, 0, AUX_SC1);
}
#define CBAR() asm volatile("" ::: "memory")
#define SB() __builtin_amdgcn_sched_barrier(0)
__device__ __forceinline__ unsigned long long rt() { return __builtin_amdgcn_s_memrealtime(); }
__device__ __forceinline__ void wait_ticks(int n) { if (n > 0) { const unsigned long long t = rt() + n; while (rt() < t) __builtin_amdgcn_s_sleep(1); } }
__device__ __forceinline__ unsigned lds_ld(LAS unsigned* p) { return __hip_atomic_load(p, __ATOMIC_RELAXED, __HIP_MEMORY_SCOPE_WORKGROUP); }
__device__ __forceinline__ void lds_st(LAS unsigned* p, unsigned v) { __hip_atomic_store(p, v, __ATOMIC_RELAXED, __HIP_MEMORY_SCOPE_WORKGROUP); }
__device__ __forceinline__ void lds_wait(LAS unsigned* p) {
  _Pragma("unroll 1") for (unsigned n = 0; n < SPIN_CAP; ++n) { CBAR(); if (lds_ld(p) != 0u) break; __builtin_amdgcn_s_sleep(1); }
  CBAR();
}
__device__ __forceinline__ unsigned lds_arrive(LAS unsigned* p, int lane) {
  CBAR();
  unsigned old = 0;
  if (lane == 0) old = __hip_atomic_fetch_add(p, 1u, __ATOMIC_RELAXED, __HIP_MEMORY_SCOPE_WORKGROUP);
  old = __builtin_amdgcn_readfirstlane(old);
  CBAR();
  return old;
}
__device__ __forceinline__ float divr(float x, float d, float r) { const float q = x * r; return fmaf(fmaf(-q, d, x), r, q); }
#define STAMP(k) do {} while (0)
#define STAMP_DECL do {} while (0)
#define STAMP_FLUSH() do {} while (0)
}

__global__ void __launch_bounds__(1024, 4)
fused_decode(const u16* __restrict__ x, const u16* __restrict__ kc, const u16* __restrict__ vc, const u16* __restrict__ wq,
             const u16* __restrict__ wo, const u16* __restrict__ wg, const u16* __restrict__ wu, const u16* __restrict__ wd,
             u16* __restrict__ out, unsigned char* __restrict__ ws) {
  __shared__ __attribute__((aligned(16))) unsigned char smem[L_END];
  LAS unsigned char* L = (LAS unsigned char*)smem;
  LAS unsigned* sync = (LAS unsigned*)(L + L_SYNC);
  const int tid = threadIdx.x, lane = tid & 63, wave = __builtin_amdgcn_readfirstlane(tid >> 6);
  const int wgid = blockIdx.x, jj = wgid >> 3, h = (wgid & 7) * 4 + (jj >> 3), j = jj & 7;
  const int g = lane >> 4, c = lane & 15;
  STAMP_DECL;
  STAMP(0);
  asm volatile("" :: "s"(x), "s"(kc), "s"(vc), "s"(wq), "s"(wo), "s"(wg), "s"(wu), "s"(wd), "s"(out), "s"(ws));
  v4u mask;
  { const unsigned half = (c & 1) ? 0xffff0000u : 0x0000ffffu; const int kk = (c & 7) >> 1;
    mask.x = kk == 0 ? half : 0u; mask.y = kk == 1 ? half : 0u; mask.z = kk == 2 ? half : 0u; mask.w = kk == 3 ? half : 0u; }
  const __amdgpu_buffer_rsrc_t rw = rsrc(ws, WS_END);
  const int h7 = h & 7, nch = 5 + ((h7 == 0 || h7 == 1 || h7 == 4) ? 1 : 0);
  const int c0 = 5 * h + 3 * (h >> 3) + (h7 > 0) + (h7 > 1) + (h7 > 4);

  if (wave < 4) {
    const int a = wave;
    if (a == 0) {
      if (lane < 16) sync[lane] = 0u;
    }
    const __amdgpu_buffer_rsrc_t rx = rsrc(x, 2048), rq = rsrc(wq, 512 * 4096 * 2);
    const v4u x0 = __builtin_amdgcn_raw_buffer_load_b128(rx, lane * 32, 0, 0), x1 = __builtin_amdgcn_raw_buffer_load_b128(rx, lane * 32 + 16, 0, 0);
    SB();
    unsigned xo = 0, ep = 0;
    if (a == 0) { xo = __builtin_amdgcn_raw_buffer_load_b32(rx, wgid * 8 + (lane & 1) * 4, 0, 0); ep = __builtin_amdgcn_raw_buffer_load_b32(rw, WS_EPOCH, 0, 0); }
    SB();
    v4u w[4];
    { const int voff = g * 8192 + c * 16, soff = (64 * j + 16 * a) * 8192 + h * 256;
#pragma unroll
      for (int i = 0; i < 4; ++i) { w[i] = ldnt(rq, voff, soff + i * 4 * 8192); SB(); } }
    asm volatile("s_waitcnt lgkmcnt(0)" ::: "memory");
    __builtin_amdgcn_s_barrier();
    CBAR();
    STAMP(1);
    unsigned tA, tB;
    *(LAS v4u*)(L + L_X + lane * 32) = x0; *(LAS v4u*)(L + L_X + lane * 32 + 16) = x1;
    float xv[16];
    { const unsigned r[8] = {x0.x, x0.y, x0.z, x0.w, x1.x, x1.y, x1.z, x1.w};
#pragma unroll
      for (int i = 0; i < 8; ++i) { xv[2 * i] = bflo(r[i]); xv[2 * i + 1] = bfhi(r[i]); } }
    float s = 0.f;
#pragma unroll
    for (int i = 0; i < 16; i += 4) s += (xv[i] + xv[i + 1]) + (xv[i + 2] + xv[i + 3]);
    s += dppf<XOR1>(s); s += dppf<XOR2>(s); s += dppf<XOR7>(s); s += dppf<MIR>(s); s = x16_sum(s);
    const float mean = s * (1.0f / 512.0f);
    float v = 0.f;
#pragma unroll
    for (int i = 0; i < 16; ++i) { const float d = xv[i] - mean; v = fmaf(d, d, v); }
    v += dppf<XOR1>(v); v += dppf<XOR2>(v); v += dppf<XOR7>(v); v += dppf<MIR>(v); v = x16_sum(v);
    const float mb = rbf(mean), den = rbf(sqrtf(rbf(rbf(v * (1.0f / 512.0f)) + 1e-5f)));
    const float m0 = __uint_as_float(__builtin_amdgcn_readlane(__float_as_uint(mb), 0)), m1 = __uint_as_float(__builtin_amdgcn_readlane(__float_as_uint(mb), 32));
    const float d0 = __uint_as_float(__builtin_amdgcn_readlane(__float_as_uint(den), 0)), d1 = __uint_as_float(__builtin_amdgcn_readlane(__float_as_uint(den), 32));
    if (a == 0) {
      asm volatile("" : "+v"(ep));
      tA = ep + 1u;
      if (lane == 0) { *(LAS f32x4*)(L + L_STAT) = f32x4{m0, d0, m1, d1}; lds_st(sync + W_TAG, tA); CBAR(); lds_st(sync + F_X, 1u); }
    }
    STAMP(2);
    {
      const int b = c >> 3; const float mm = b ? m1 : m0, dd = b ? d1 : d0, rr = 1.0f / dd;
      f32x4 acc = {0.f, 0.f, 0.f, 0.f};
#pragma unroll
      for (int i = 0; i < 4; ++i) {
        const unsigned raw = *(LAS u16*)(L + L_X + (b * 512 + 64 * j + 16 * a + 4 * i + g) * 2);
        const float xn = divr(rbf(bf2f(raw) - mm), dd, rr);
        const unsigned pk = cvtpk(xn, xn);
        acc = mfma(mask & pk, w[i], acc);
      }
      *(LAS f32x4*)(L + L_RED1 + (a * 64 + lane) * 16) = acc;
    }
    STAMP(3);
    if (lds_arrive(sync + C_A, lane) == 3u) {
      tA = lds_ld(sync + W_TAG); tB = ~tA;
      f32x4 t = *(LAS f32x4*)(L + L_RED1 + lane * 16);
#pragma unroll
      for (int k = 1; k < 4; ++k) t += *(LAS f32x4*)(L + L_RED1 + (k * 64 + lane) * 16);
      const int po = WS_S1Q + (h * 8 + j) * 2048 + ((g >> 1) * 64 + 4 * c + 2 * (g & 1)) * 16;
      stc(rw, po, t.x, t.y, tA, tB); stc(rw, po + 16, t.z, t.w, tA, tB);
      STAMP(4);
      wait_ticks(DELAY1);
      const int go = WS_S1Q + h * 8 * 2048 + ((j >> 2) * 64 + lane) * 16;
      float q0, q1;
      _Pragma("unroll 1") for (unsigned n = 0; n < SPIN_CAP; ++n) {
        CBAR();
        v4u p[8];
#pragma unroll
        for (int k = 0; k < 8; ++k) p[k] = ldc(rw, go + k * 2048);
        bool ok = true; q0 = 0.f; q1 = 0.f;
#pragma unroll
        for (int k = 0; k < 8; ++k) { ok &= (p[k].y == tA) & (p[k].w == tB); q0 += __uint_as_float(p[k].x); q1 += __uint_as_float(p[k].z); }
        if (__all(ok)) break;
      }
      *(LAS unsigned*)(L + L_Q + lane * 4) = cvtpk(q0, q1);
      CBAR();
      if (lane == 0) lds_st(sync + F_Q, 1u);
      STAMP(5);
    }
    if (a == 0) {
      lds_wait(sync + F_C);
      STAMP(6);
      tA = ep + 1u; tB = ~tA;
      wait_ticks(DELAY3);
      float y0, y1;
      _Pragma("unroll 1") for (unsigned n = 0; n < SPIN_CAP; ++n) {
        CBAR();
        const v4u p0 = ldc(rw, WS_S3 + wgid * 2048 + lane * 16), p1 = ldc(rw, WS_S3 + wgid * 2048 + 1024 + lane * 16);
        const bool ok = (p0.y == tA) & (p0.w == tB) & (p1.y == tA) & (p1.w == tB);
        y0 = __uint_as_float(p0.x) + __uint_as_float(p1.x); y1 = __uint_as_float(p0.z) + __uint_as_float(p1.z);
        if (__all(ok)) break;
      }
      STAMP(7);
      y0 += dppf<XOR2>(y0); y1 += dppf<XOR2>(y1);
      y0 += dppf<ROR4>(y0); y1 += dppf<ROR4>(y1);
      y0 += dppf<ROR8>(y0); y1 += dppf<ROR8>(y1);
      y0 = x32_sum(x16_sum(y0)); y1 = x32_sum(x16_sum(y1));
      if (lane < 2) ((unsigned*)out)[wgid * 2 + lane] = cvtpk(bflo(xo) + y0, bfhi(xo) + y1);
      if (wgid == 0 && lane == 0) __builtin_amdgcn_raw_buffer_store_b32(tA, rw, WS_EPOCH, 0, AUX_SC1);
      STAMP(8);
    }
  } else if (wave < 12) {
    const int bw = wave - 4;
    __builtin_amdgcn_s_barrier();
    CBAR();
    const __amdgpu_buffer_rsrc_t rk = rsrc(kc, 2u * 32 * 4096 * 128 * 2), rv = rsrc(vc, 2u * 32 * 4096 * 128 * 2);
    const int soff = (((j >> 2) * 32 + h) * 4096 + 3072 + (j & 3) * 256 + bw * 32) * 256;
    v4u K[8], V[8];
#pragma unroll
    for (int i = 0; i < 8; ++i) { K[i] = ldnt(rk, lane * 16 + (i & 3) * 1024, soff + (i >> 2) * 4096); SB(); }
#pragma unroll
    for (int i = 0; i < 8; ++i) { V[i] = ldnt(rv, lane * 16 + (i & 3) * 1024, soff + (i >> 2) * 4096); SB(); }
    STAMP(1);
    lds_wait(sync + F_Q);
    STAMP(2);
    const v4u q = *(LAS v4u*)(L + L_Q + c * 16);
    float sc[8];
#pragma unroll
    for (int i = 0; i < 8; ++i) sc[i] = dot2(K[i].w, q.w, dot2(K[i].z, q.z, dot2(K[i].y, q.y, dot2(K[i].x, q.x, 0.f))));
    { const bool b3 = c & 8, b2 = c & 4, b1 = c & 2;
#pragma unroll
      for (int i = 0; i < 4; ++i) { const float keep = b3 ? sc[i + 4] : sc[i], send = b3 ? sc[i] : sc[i + 4]; sc[i] = keep + dppf<ROR8>(send); }
#pragma unroll
      for (int i = 0; i < 2; ++i) { const float keep = b2 ? sc[i + 2] : sc[i], send = b2 ? sc[i] : sc[i + 2]; sc[i] = keep + dppf<XOR7>(send); }
      { const float keep = b1 ? sc[1] : sc[0], send = b1 ? sc[0] : sc[1]; sc[0] = keep + dppf<XOR2>(send); }
      sc[0] += dppf<XOR1>(sc[0]); }
    const float sv = rbf(rbf(sc[0]) * 0.08838834764831845f);
    float m = fmaxf(sv, dppf<XOR2>(sv)); m = fmaxf(m, dppf<ROR4>(m)); m = fmaxf(m, dppf<ROR8>(m)); m = x32_max(x16_max(m));
    const float p = __builtin_amdgcn_exp2f((sv - m) * 1.4426950408889634f);
    float l = p + dppf<XOR2>(p); l += dppf<ROR4>(l); l += dppf<ROR8>(l); l = x32_sum(x16_sum(l));
    const unsigned ppk = cvtpk(p, p);
    const v4u mk = (c & 8) ? v4u{0u, 0u, 0u, 0u} : mask;
    STAMP(3);
    f32x4 acc = {0.f, 0.f, 0.f, 0.f};
#define PVSTEP(i) acc = mfma(mk & dppu<NEWB + 2 * (i)>(ppk), V[i], acc);
    PVSTEP(0) PVSTEP(1) PVSTEP(2) PVSTEP(3) PVSTEP(4) PVSTEP(5) PVSTEP(6) PVSTEP(7)
#undef PVSTEP
    if (lane < 32) *(LAS f32x4*)(L + L_OB + bw * 512 + (8 * c + 4 * g) * 4) = acc;
    if (lane == 0) *(LAS f32x2*)(L + L_ML + bw * 8) = f32x2{m, l};
    STAMP(4);
    if (lds_arrive(sync + C_B, lane) == 7u) {
      const unsigned tA = lds_ld(sync + W_TAG), tB = ~tA;
      f32x2 ml[8], o[8];
#pragma unroll
      for (int k = 0; k < 8; ++k) { ml[k] = *(LAS f32x2*)(L + L_ML + k * 8); o[k] = *(LAS f32x2*)(L + L_OB + k * 512 + lane * 8); }
      float M = ml[0].x;
#pragma unroll
      for (int k = 1; k < 8; ++k) M = fmaxf(M, ml[k].x);
      float Ls = 0.f, o0 = 0.f, o1 = 0.f;
#pragma unroll
      for (int k = 0; k < 8; ++k) { const float e = __builtin_amdgcn_exp2f((ml[k].x - M) * 1.4426950408889634f); Ls = fmaf(e, ml[k].y, Ls); o0 = fmaf(e, o[k].x, o0); o1 = fmaf(e, o[k].y, o1); }
      const int po = WS_S2 + (h * 8 + j) * 2048;
      stc(rw, po + lane * 16, o0, o1, tA, tB);
      if (lane == 0) stc(rw, po + 1024, M, Ls, tA, tB);
      STAMP(5);
    }
  } else {
    const int w = wave - 12;
    __builtin_amdgcn_s_barrier();
    CBAR();
    __builtin_amdgcn_s_setprio(3);
    const int mm_ = j & 1, rq = j >> 1;
    const int cq = j & 3, rh = j >> 2;
    const __amdgpu_buffer_rsrc_t rg = rsrc(mm_ ? wu : wg, 512 * 1376 * 2), ro = rsrc(wo, 4096 * 512 * 2), rd = rsrc(wd, 1376 * 512 * 2);
    v4u G[4], O[4], Dn[2];
    { const int p = c & 7; const int voff = p < nch ? (2 * g + (c >> 3)) * 2752 + p * 16 : 0x7fffffff - (1 << 24);
      const int soff = (128 * rq + 32 * w) * 2752 + c0 * 16;
#pragma unroll
      for (int i = 0; i < 4; ++i) { G[i] = ldnt(rg, voff, soff + i * 8 * 2752); SB(); } }
    __builtin_amdgcn_s_setprio(0);
    { const int voff = g * 1024 + c * 16, soff = (128 * h + 64 * rh + 16 * w) * 1024 + cq * 256;
#pragma unroll
      for (int i = 0; i < 4; ++i) { O[i] = ldnt(ro, voff, soff + i * 4096); SB(); }
      const int so2 = (8 * c0 + rh * 4 * nch + 4 * w) * 1024 + cq * 256;
      Dn[0] = ldnt(rd, voff, so2); SB();
      Dn[1] = ldnt(rd, (w + 4 < nch) ? voff : 0x7fffffff - (1 << 24), so2 + 16 * 1024); SB(); }
    STAMP(1);
    lds_wait(sync + F_X);
    const unsigned tA = lds_ld(sync + W_TAG), tB = ~tA;
    const f32x4 st = *(LAS f32x4*)(L + L_STAT);
    { const float r0 = 1.0f / st.y, r1 = 1.0f / st.w;
      f32x4 a0 = {0.f, 0.f, 0.f, 0.f}, a1 = {0.f, 0.f, 0.f, 0.f};
#pragma unroll
      for (int i = 0; i < 4; ++i) {
        const int row = 128 * rq + 32 * w + 8 * i + 2 * g + (c >> 3);
        const unsigned raw0 = *(LAS u16*)(L + L_X + row * 2), raw1 = *(LAS u16*)(L + L_X + (512 + row) * 2);
        const float n0 = divr(rbf(bf2f(raw0) - st.x), st.y, r0), n1 = divr(rbf(bf2f(raw1) - st.z), st.w, r1);
        a0 = mfma(mask & cvtpk(n0, n0), G[i], a0);
        a1 = mfma(mask & cvtpk(n1, n1), G[i], a1);
      }
      if ((g >> 1) == (c >> 3) && (c & 7) < 6) {
        const int o = L_REDM + ((w * 2 + (c >> 3)) * 2 * 48 + 8 * (c & 7) + 4 * (g & 1)) * 4;
        *(LAS f32x4*)(L + o) = a0; *(LAS f32x4*)(L + o + 48 * 4) = a1;
      } }
    STAMP(2);
    if (lds_arrive(sync + C_M, lane) == 3u) {
      if (lane < 48) {
        f32x2 t = {0.f, 0.f};
#pragma unroll
        for (int k = 0; k < 8; ++k) t += *(LAS f32x2*)(L + L_REDM + k * 384 + lane * 8);
        stc(rw, WS_S1M + (h * 8 + j) * 1024 + lane * 16, t.x, t.y, tA, tB);
      }
    }
    STAMP(3);
    wait_ticks(DELAYM);
    unsigned hpk0, hpk1;
    { const int b = c >> 3, dq = (c >> 2) & 1; const int fl = rh * 4 * nch + 4 * (w + 4 * dq) + g;
      const bool act = (w + 4 * dq) < nch;
      const int go = WS_S1M + (h * 8 + 2 * (c & 3)) * 1024 + (b * 24 + (fl >> 1)) * 16;
      float gv, uv;
      _Pragma("unroll 1") for (unsigned n = 0; n < SPIN_CAP; ++n) {
        CBAR();
        const v4u pg = ldc(rw, go), pu = ldc(rw, go + 1024);
        const bool ok = !act || ((pg.y == tA) & (pg.w == tB) & (pu.y == tA) & (pu.w == tB));
        gv = __uint_as_float((g & 1) ? pg.z : pg.x); uv = __uint_as_float((g & 1) ? pu.z : pu.x);
        if (__all(ok)) break;
      }
      gv += dppf<XOR1>(gv); uv += dppf<XOR1>(uv); gv += dppf<XOR2>(gv); uv += dppf<XOR2>(uv);
      gv = rbf(gv); uv = rbf(uv);
      const float sg = rbf(1.0f / (1.0f + __builtin_amdgcn_exp2f(-gv * 1.4426950408889634f)));
      const float hv = act ? sg * uv : 0.f;
      const unsigned hp = cvtpk(hv, hv);
      hpk0 = (unsigned)__builtin_amdgcn_update_dpp((int)hp, (int)hp, 0x114, 0xf, 0xa, false);
      hpk1 = (unsigned)__builtin_amdgcn_update_dpp((int)hp, (int)hp, 0x104, 0xf, 0x5, false);
    }
    STAMP(4);
    f32x4 acc = {0.f, 0.f, 0.f, 0.f};
    acc = mfma(mask & hpk0, Dn[0], acc);
    acc = mfma(mask & hpk1, Dn[1], acc);
    {
      const int b = c >> 3, dim = 64 * rh + 16 * w + 4 * (c & 3) + g;
      const int go = WS_S2 + (h * 8 + b * 4) * 2048;
      wait_ticks(DELAY2);
      float ov[4], mv, lv;
      _Pragma("unroll 1") for (unsigned n = 0; n < SPIN_CAP; ++n) {
        CBAR();
        v4u p[4];
#pragma unroll
        for (int s = 0; s < 4; ++s) p[s] = ldc(rw, go + s * 2048 + (dim >> 1) * 16);
        const v4u pm = ldc(rw, go + (c & 3) * 2048 + 1024);
        bool ok = (pm.y == tA) & (pm.w == tB);
#pragma unroll
        for (int s = 0; s < 4; ++s) { ok &= (p[s].y == tA) & (p[s].w == tB); ov[s] = __uint_as_float((g & 1) ? p[s].z : p[s].x); }
        mv = __uint_as_float(pm.x); lv = __uint_as_float(pm.z);
        if (__all(ok)) break;
      }
      STAMP(5);
      float M = fmaxf(mv, dppf<XOR1>(mv)); M = fmaxf(M, dppf<XOR2>(M));
      const float e = __builtin_amdgcn_exp2f((mv - M) * 1.4426950408889634f);
      float Ls = e * lv; Ls += dppf<XOR1>(Ls); Ls += dppf<XOR2>(Ls);
      const float wt = e / Ls;
      float o = dppf<0x00>(wt) * ov[0];
      o = fmaf(dppf<0x55>(wt), ov[1], o); o = fmaf(dppf<0xAA>(wt), ov[2], o); o = fmaf(dppf<0xFF>(wt), ov[3], o);
      const unsigned opk = cvtpk(o, o);
      acc = mfma(mask & dppu<0x00>(opk), O[0], acc);
      acc = mfma(mask & dppu<0x55>(opk), O[1], acc);
      acc = mfma(mask & dppu<0xAA>(opk), O[2], acc);
      acc = mfma(mask & dppu<0xFF>(opk), O[3], acc);
    }
    *(LAS f32x4*)(L + L_RED2 + (w * 64 + lane) * 16) = acc;
    STAMP(6);
    if (lds_arrive(sync + C_2, lane) == 3u) {
      f32x4 t = *(LAS f32x4*)(L + L_RED2 + lane * 16);
#pragma unroll
      for (int k = 1; k < 4; ++k) t += *(LAS f32x4*)(L + L_RED2 + (k * 64 + lane) * 16);
      const int cg = (g >> 1) * 128 + 32 * cq + 2 * c + (g & 1);
      const int po = WS_S3 + cg * 2048 + (2 * h + rh) * 32;
      stc(rw, po, t.x, t.y, tA, tB); stc(rw, po + 16, t.z, t.w, tA, tB);
      CBAR();
      if (lane == 0) lds_st(sync + F_C, 1u);
      STAMP(7);
    }
  }
  STAMP_FLUSH();
}

extern "C" void kernel_launch(void* const* d_in, const int* in_sizes, int n_in, void* d_out, int out_size, void* d_ws, size_t ws_size, hipStream_t stream) {
  fused_decode<<<256, 1024, 0, stream>>>((const u16*)d_in[0], (const u16*)d_in[1], (const u16*)d_in[2], (const u16*)d_in[3], (const u16*)d_in[4],
                                         (const u16*)d_in[6], (const u16*)d_in[7], (const u16*)d_in[8], (u16*)d_out, (unsigned char*)d_ws);
}
```

```cpp
#include <hip/hip_runtime.h>
#include <stdint.h>

typedef unsigned short u16;
typedef unsigned v4u __attribute__((ext_vector_type(4)));
typedef unsigned v2u __attribute__((ext_vector_type(2)));
typedef float f32x4 __attribute__((ext_vector_type(4)));
typedef float f32x2 __attribute__((ext_vector_type(2)));
typedef short bf16x8 __attribute__((ext_vector_type(8)));
typedef __bf16 bf16x2 __attribute__((ext_vector_type(2)));
#define LAS __attribute__((address_space(3)))

#define DELAY1 60
#define DELAYM 40
#define DELAY2 0
#define DELAY3 30
#define CPRIO 0

namespace {
constexpr int AUX_NT = 18, AUX_SC1 = 16;
constexpr unsigned RSRC3 = 0x00020000u;
constexpr int WS_EPOCH = 0;
constexpr int WS_S1Q = 4096;
constexpr int WS_S1M = WS_S1Q + 256 * 2048;
constexpr int WS_S2 = WS_S1M + 256 * 1024;
constexpr int WS_S3 = WS_S2 + 256 * 2048;
constexpr int WS_END = WS_S3 + 256 * 2048;
constexpr int WS_STAMP = 8 << 20;
constexpr int L_SYNC = 0;
constexpr int L_STAT = 64;
constexpr int L_Q = 128;
constexpr int L_X = 512;
constexpr int L_RED1 = 2560;
constexpr int L_REDM = L_RED1 + 4096;
constexpr int L_RED2 = L_REDM + 3072;
constexpr int L_OB = L_RED2 + 4096;
constexpr int L_ML = L_OB + 4096;
constexpr int L_END = L_ML + 64;
enum { C_A = 0, C_B = 1, C_M = 2, C_2 = 3, F_Q = 4, F_X = 5, F_C = 6, W_TAG = 7 };
constexpr unsigned SPIN_CAP = 1u << 22;

__device__ __forceinline__ float bf2f(unsigned v) { return __uint_as_float(v << 16); }
__device__ __forceinline__ float bflo(unsigned v) { return __uint_as_float(v << 16); }
__device__ __forceinline__ float bfhi(unsigned v) { return __uint_as_float(v & 0xffff0000u); }
__device__ __forceinline__ unsigned cvtpk(float a, float b) { f32x2 v = {a, b}; return __builtin_bit_cast(unsigned, __builtin_convertvector(v, bf16x2)); }
__device__ __forceinline__ float rbf(float a) { return bflo(cvtpk(a, a)); }
template <int CTRL> __device__ __forceinline__ unsigned dppu(unsigned x) { return (unsigned)__builtin_amdgcn_mov_dpp((int)x, CTRL, 0xf, 0xf, true); }
template <int CTRL> __device__ __forceinline__ float dppf(float x) { return __uint_as_float(dppu<CTRL>(__float_as_uint(x))); }
constexpr int XOR1 = 0xB1, XOR2 = 0x4E, XOR7 = 0x141, MIR = 0x140, ROR4 = 0x124, ROR8 = 0x128, NEWB = 0x150;
__device__ __forceinline__ float x16_sum(float x) { auto s = __builtin_amdgcn_permlane16_swap(__float_as_uint(x), __float_as_uint(x), false, false); return __uint_as_float(s[0]) + __uint_as_float(s[1]); }
__device__ __forceinline__ float x32_sum(float x) { auto s = __builtin_amdgcn_permlane32_swap(__float_as_uint(x), __float_as_uint(x), false, false); return __uint_as_float(s[0]) + __uint_as_float(s[1]); }
__device__ __forceinline__ float x16_max(float x) { auto s = __builtin_amdgcn_permlane16_swap(__float_as_uint(x), __float_as_uint(x), false, false); return fmaxf(__uint_as_float(s[0]), __uint_as_float(s[1])); }
__device__ __forceinline__ float x32_max(float x) { auto s = __builtin_amdgcn_permlane32_swap(__float_as_uint(x), __float_as_uint(x), false, false); return fmaxf(__uint_as_float(s[0]), __uint_as_float(s[1])); }
__device__ __forceinline__ f32x4 mfma(v4u a, v4u b, f32x4 c) { return __builtin_amdgcn_mfma_f32_16x16x32_bf16(__builtin_bit_cast(bf16x8, a), __builtin_bit_cast(bf16x8, b), c, 0, 0, 0); }
__device__ __forceinline__ float dot2(unsigned a, unsigned b, float c) { return __builtin_amdgcn_fdot2_f32_bf16(__builtin_bit_cast(bf16x2, a), __builtin_bit_cast(bf16x2, b), c, false); }
__device__ __forceinline__ __amdgpu_buffer_rsrc_t rsrc(const void* p, unsigned bytes) { return __builtin_amdgcn_make_buffer_rsrc((void*)p, 0, (int)bytes, RSRC3); }
__device__ __forceinline__ v4u ldnt(__amdgpu_buffer_rsrc_t r, int voff, int soff) { return __builtin_amdgcn_raw_buffer_load_b128(r, voff, soff, AUX_NT); }
__device__ __forceinline__ v4u ldc(__amdgpu_buffer_rsrc_t r, int voff) { return __builtin_amdgcn_raw_buffer_load_b128(r, voff, 0, AUX_SC1); }
__device__ __forceinline__ void stc(__amdgpu_buffer_rsrc_t r, int voff, float a, float b, unsigned tA, unsigned tB) {
  v4u v = {__float_as_uint(a), tA, __float_as_uint(b), tB};
  __builtin_amdgcn_raw_buffer_store_b128(v, r, voff, 0, AUX_SC1);
}
#define CBAR() asm volatile("" ::: "memory")
#define SB() __builtin_amdgcn_sched_barrier(0)
__device__ __forceinline__ unsigned long long rt() { return __builtin_amdgcn_s_memrealtime(); }
__device__ __forceinline__ void wait_ticks(int n) { if (n > 0) { const unsigned long long t = rt() + n; while (rt() < t) __builtin_amdgcn_s_sleep(1); } }
__device__ __forceinline__ unsigned lds_ld(LAS unsigned* p) { return __hip_atomic_load(p, __ATOMIC_RELAXED, __HIP_MEMORY_SCOPE_WORKGROUP); }
__device__ __forceinline__ void lds_st(LAS unsigned* p, unsigned v) { __hip_atomic_store(p, v, __ATOMIC_RELAXED, __HIP_MEMORY_SCOPE_WORKGROUP); }
__device__ __forceinline__ void lds_wait(LAS unsigned* p) {
  _Pragma("unroll 1") for (unsigned n = 0; n < SPIN_CAP; ++n) { CBAR(); if (lds_ld(p) != 0u) break; __builtin_amdgcn_s_sleep(1); }
  CBAR();
}
__device__ __forceinline__ unsigned lds_arrive(LAS unsigned* p, int lane) {
  CBAR();
  unsigned old = 0;
  if (lane == 0) old = __hip_atomic_fetch_add(p, 1u, __ATOMIC_RELAXED, __HIP_MEMORY_SCOPE_WORKGROUP);
  old = __builtin_amdgcn_readfirstlane(old);
  CBAR();
  return old;
}
__device__ __forceinline__ float divr(float x, float d, float r) { const float q = x * r; return fmaf(fmaf(-q, d, x), r, q); }
#define STAMP(k) do {} while (0)
#define STAMP_DECL do {} while (0)
#define STAMP_FLUSH() do {} while (0)
}

__global__ void __launch_bounds__(1024, 4)
fused_decode(const u16* __restrict__ x, const u16* __restrict__ kc, const u16* __restrict__ vc, const u16* __restrict__ wq,
             const u16* __restrict__ wo, const u16* __restrict__ wg, const u16* __restrict__ wu, const u16* __restrict__ wd,
             u16* __restrict__ out, unsigned char* __restrict__ ws) {
  __shared__ __attribute__((aligned(16))) unsigned char smem[L_END];
  LAS unsigned char* L = (LAS unsigned char*)smem;
  LAS unsigned* sync = (LAS unsigned*)(L + L_SYNC);
  const int tid = threadIdx.x, lane = tid & 63, wave = __builtin_amdgcn_readfirstlane(tid >> 6);
  const int wgid = blockIdx.x, jj = wgid >> 3, h = (wgid & 7) * 4 + (jj >> 3), j = jj & 7;
  const int g = lane >> 4, c = lane & 15;
  STAMP_DECL;
  STAMP(0);
  asm volatile("" :: "s"(x), "s"(kc), "s"(vc), "s"(wq), "s"(wo), "s"(wg), "s"(wu), "s"(wd), "s"(out), "s"(ws));
  v4u mask;
  { const unsigned half = (c & 1) ? 0xffff0000u : 0x0000ffffu; const int kk = (c & 7) >> 1;
    mask.x = kk == 0 ? half : 0u; mask.y = kk == 1 ? half : 0u; mask.z = kk == 2 ? half : 0u; mask.w = kk == 3 ? half : 0u; }
  const __amdgpu_buffer_rsrc_t rw = rsrc(ws, WS_END);
  const int h7 = h & 7, nch = 5 + ((h7 == 0 || h7 == 1 || h7 == 4) ? 1 : 0);
  const int c0 = 5 * h + 3 * (h >> 3) + (h7 > 0) + (h7 > 1) + (h7 > 4);

  if (wave < 4) {
    const int a = wave;
    if (a == 0) {
      if (lane < 16) sync[lane] = 0u;
    }
    const __amdgpu_buffer_rsrc_t rx = rsrc(x, 2048), rq = rsrc(wq, 512 * 4096 * 2);
    const v4u x0 = __builtin_amdgcn_raw_buffer_load_b128(rx, lane * 32, 0, 0), x1 = __builtin_amdgcn_raw_buffer_load_b128(rx, lane * 32 + 16, 0, 0);
    SB();
    unsigned xo = 0, ep = 0;
    if (a == 0) { xo = __builtin_amdgcn_raw_buffer_load_b32(rx, wgid * 8 + (lane & 1) * 4, 0, 0); ep = __builtin_amdgcn_raw_buffer_load_b32(rw, WS_EPOCH, 0, 0); }
    SB();
    v4u w[4];
    { const int voff = g * 8192 + c * 16, soff = (64 * j + 16 * a) * 8192 + h * 256;
#pragma unroll
      for (int i = 0; i < 4; ++i) { w[i] = ldnt(rq, voff, soff + i * 4 * 8192); SB(); } }
    asm volatile("s_waitcnt lgkmcnt(0)" ::: "memory");
    __builtin_amdgcn_s_barrier();
    CBAR();
    STAMP(1);
    unsigned tA, tB;
    *(LAS v4u*)(L + L_X + lane * 32) = x0; *(LAS v4u*)(L + L_X + lane * 32 + 16) = x1;
    float xv[16];
    { const unsigned r[8] = {x0.x, x0.y, x0.z, x0.w, x1.x, x1.y, x1.z, x1.w};
#pragma unroll
      for (int i = 0; i < 8; ++i) { xv[2 * i] = bflo(r[i]); xv[2 * i + 1] = bfhi(r[i]); } }
    float s = 0.f;
#pragma unroll
    for (int i = 0; i < 16; i += 4) s += (xv[i] + xv[i + 1]) + (xv[i + 2] + xv[i + 3]);
    s += dppf<XOR1>(s); s += dppf<XOR2>(s); s += dppf<XOR7>(s); s += dppf<MIR>(s); s = x16_sum(s);
    const float mean = s * (1.0f / 512.0f);
    float v = 0.f;
#pragma unroll
    for (int i = 0; i < 16; ++i) { const float d = xv[i] - mean; v = fmaf(d, d, v); }
    v += dppf<XOR1>(v); v += dppf<XOR2>(v); v += dppf<XOR7>(v); v += dppf<MIR>(v); v = x16_sum(v);
    const float mb = rbf(mean), den = rbf(sqrtf(rbf(rbf(v * (1.0f / 512.0f)) + 1e-5f)));
    const float m0 = __uint_as_float(__builtin_amdgcn_readlane(__float_as_uint(mb), 0)), m1 = __uint_as_float(__builtin_amdgcn_readlane(__float_as_uint(mb), 32));
    const float d0 = __uint_as_float(__builtin_amdgcn_readlane(__float_as_uint(den), 0)), d1 = __uint_as_float(__builtin_amdgcn_readlane(__float_as_uint(den), 32));
    if (a == 0) {
      asm volatile("" : "+v"(ep));
      tA = ep + 1u;
      if (lane == 0) { *(LAS f32x4*)(L + L_STAT) = f32x4{m0, d0, m1, d1}; lds_st(sync + W_TAG, tA); CBAR(); lds_st(sync + F_X, 1u); }
    }
    STAMP(2);
    {
      const int b = c >> 3; const float mm = b ? m1 : m0, dd = b ? d1 : d0, rr = 1.0f / dd;
      f32x4 acc = {0.f, 0.f, 0.f, 0.f};
#pragma unroll
      for (int i = 0; i < 4; ++i) {
        const unsigned raw = *(LAS u16*)(L + L_X + (b * 512 + 64 * j + 16 * a + 4 * i + g) * 2);
        const float xn = divr(rbf(bf2f(raw) - mm), dd, rr);
        const unsigned pk = cvtpk(xn, xn);
        acc = mfma(mask & pk, w[i], acc);
      }
      *(LAS f32x4*)(L + L_RED1 + (a * 64 + lane) * 16) = acc;
    }
    STAMP(3);
    if (lds_arrive(sync + C_A, lane) == 3u) {
      tA = lds_ld(sync + W_TAG); tB = ~tA;
      f32x4 t = *(LAS f32x4*)(L + L_RED1 + lane * 16);
#pragma unroll
      for (int k = 1; k < 4; ++k) t += *(LAS f32x4*)(L + L_RED1 + (k * 64 + lane) * 16);
      const int po = WS_S1Q + (h * 8 + j) * 2048 + ((g >> 1) * 64 + 4 * c + 2 * (g & 1)) * 16;
      stc(rw, po, t.x, t.y, tA, tB); stc(rw, po + 16, t.z, t.w, tA, tB);
      STAMP(4);
      wait_ticks(DELAY1);
      const int go = WS_S1Q + h * 8 * 2048 + ((j >> 2) * 64 + lane) * 16;
      float q0, q1;
      _Pragma("unroll 1") for (unsigned n = 0; n < SPIN_CAP; ++n) {
        CBAR();
        v4u p[8];
#pragma unroll
        for (int k = 0; k < 8; ++k) p[k] = ldc(rw, go + k * 2048);
        bool ok = true; q0 = 0.f; q1 = 0.f;
#pragma unroll
        for (int k = 0; k < 8; ++k) { ok &= (p[k].y == tA) & (p[k].w == tB); q0 += __uint_as_float(p[k].x); q1 += __uint_as_float(p[k].z); }
        if (__all(ok)) break;
      }
      *(LAS unsigned*)(L + L_Q + lane * 4) = cvtpk(q0, q1);
      CBAR();
      if (lane == 0) lds_st(sync + F_Q, 1u);
      STAMP(5);
    }
    if (a == 0) {
      lds_wait(sync + F_C);
      STAMP(6);
      tA = ep + 1u; tB = ~tA;
      wait_ticks(DELAY3);
      float y0, y1;
      _Pragma("unroll 1") for (unsigned n = 0; n < SPIN_CAP; ++n) {
        CBAR();
        const v4u p0 = ldc(rw, WS_S3 + wgid * 2048 + lane * 16), p1 = ldc(rw, WS_S3 + wgid * 2048 + 1024 + lane * 16);
        const bool ok = (p0.y == tA) & (p0.w == tB) & (p1.y == tA) & (p1.w == tB);
        y0 = __uint_as_float(p0.x) + __uint_as_float(p1.x); y1 = __uint_as_float(p0.z) + __uint_as_float(p1.z);
        if (__all(ok)) break;
      }
      STAMP(7);
      y0 += dppf<XOR2>(y0); y1 += dppf<XOR2>(y1);
      y0 += dppf<ROR4>(y0); y1 += dppf<ROR4>(y1);
      y0 += dppf<ROR8>(y0); y1 += dppf<ROR8>(y1);
      y0 = x32_sum(x16_sum(y0)); y1 = x32_sum(x16_sum(y1));
      if (lane < 2) ((unsigned*)out)[wgid * 2 + lane] = cvtpk(bflo(xo) + y0, bfhi(xo) + y1);
      if (wgid == 0 && lane == 0) __builtin_amdgcn_raw_buffer_store_b32(tA, rw, WS_EPOCH, 0, AUX_SC1);
      STAMP(8);
    }
  } else if (wave < 12) {
    const int bw = wave - 4;
    __builtin_amdgcn_s_barrier();
    CBAR();
    const __amdgpu_buffer_rsrc_t rk = rsrc(kc, 2u * 32 * 4096 * 128 * 2), rv = rsrc(vc, 2u * 32 * 4096 * 128 * 2);
    const int soff = (((j >> 2) * 32 + h) * 4096 + 3072 + (j & 3) * 256 + bw * 32) * 256;
    v4u K[8], V[8];
#pragma unroll
    for (int i = 0; i < 8; ++i) { K[i] = ldnt(rk, lane * 16 + (i & 3) * 1024, soff + (i >> 2) * 4096); SB(); }
#pragma unroll
    for (int i = 0; i < 8; ++i) { V[i] = ldnt(rv, lane * 16 + (i & 3) * 1024, soff + (i >> 2) * 4096); SB(); }
    STAMP(1);
    lds_wait(sync + F_Q);
    STAMP(2);
    const v4u q = *(LAS v4u*)(L + L_Q + c * 16);
    float sc[8];
#pragma unroll
    for (int i = 0; i < 8; ++i) sc[i] = dot2(K[i].w, q.w, dot2(K[i].z, q.z, dot2(K[i].y, q.y, dot2(K[i].x, q.x, 0.f))));
    { const bool b3 = c & 8, b2 = c & 4, b1 = c & 2;
#pragma unroll
      for (int i = 0; i < 4; ++i) { const float keep = b3 ? sc[i + 4] : sc[i], send = b3 ? sc[i] : sc[i + 4]; sc[i] = keep + dppf<ROR8>(send); }
#pragma unroll
      for (int i = 0; i < 2; ++i) { const float keep = b2 ? sc[i + 2] : sc[i], send = b2 ? sc[i] : sc[i + 2]; sc[i] = keep + dppf<XOR7>(send); }
      { const float keep = b1 ? sc[1] : sc[0], send = b1 ? sc[0] : sc[1]; sc[0] = keep + dppf<XOR2>(send); }
      sc[0] += dppf<XOR1>(sc[0]); }
    const float sv = rbf(rbf(sc[0]) * 0.08838834764831845f);
    float m = fmaxf(sv, dppf<XOR2>(sv)); m = fmaxf(m, dppf<ROR4>(m)); m = fmaxf(m, dppf<ROR8>(m)); m = x32_max(x16_max(m));
    const float p = __builtin_amdgcn_exp2f((sv - m) * 1.4426950408889634f);
    float l = p + dppf<XOR2>(p); l += dppf<ROR4>(l); l += dppf<ROR8>(l); l = x32_sum(x16_sum(l));
    const unsigned ppk = cvtpk(p, p);
    const v4u mk = (c & 8) ? v4u{0u, 0u, 0u, 0u} : mask;
    STAMP(3);
    f32x4 acc = {0.f, 0.f, 0.f, 0.f};
#define PVSTEP(i) acc = mfma(mk & dppu<NEWB + 2 * (i)>(ppk), V[i], acc);
    PVSTEP(0) PVSTEP(1) PVSTEP(2) PVSTEP(3) PVSTEP(4) PVSTEP(5) PVSTEP(6) PVSTEP(7)
#undef PVSTEP
    if (lane < 32) *(LAS f32x4*)(L + L_OB + bw * 512 + (8 * c + 4 * g) * 4) = acc;
    if (lane == 0) *(LAS f32x2*)(L + L_ML + bw * 8) = f32x2{m, l};
    STAMP(4);
    if (lds_arrive(sync + C_B, lane) == 7u) {
      const unsigned tA = lds_ld(sync + W_TAG), tB = ~tA;
      f32x2 ml[8], o[8];
#pragma unroll
      for (int k = 0; k < 8; ++k) { ml[k] = *(LAS f32x2*)(L + L_ML + k * 8); o[k] = *(LAS f32x2*)(L + L_OB + k * 512 + lane * 8); }
      float M = ml[0].x;
#pragma unroll
      for (int k = 1; k < 8; ++k) M = fmaxf(M, ml[k].x);
      float Ls = 0.f, o0 = 0.f, o1 = 0.f;
#pragma unroll
      for (int k = 0; k < 8; ++k) { const float e = __builtin_amdgcn_exp2f((ml[k].x - M) * 1.4426950408889634f); Ls = fmaf(e, ml[k].y, Ls); o0 = fmaf(e, o[k].x, o0); o1 = fmaf(e, o[k].y, o1); }
      const int po = WS_S2 + (h * 8 + j) * 2048;
      stc(rw, po + lane * 16, o0, o1, tA, tB);
      if (lane == 0) stc(rw, po + 1024, M, Ls, tA, tB);
      STAMP(5);
    }
  } else {
    const int w = wave - 12;
    __builtin_amdgcn_s_barrier();
    CBAR();
    const int mm_ = j & 1, rq = j >> 1;
    const int cq = j & 3, rh = j >> 2;
    const __amdgpu_buffer_rsrc_t rg = rsrc(mm_ ? wu : wg, 512 * 1376 * 2), ro = rsrc(wo, 4096 * 512 * 2), rd = rsrc(wd, 1376 * 512 * 2);
    v4u G[4], O[4], Dn[2];
    { const int p = c & 7; const int voff = p < nch ? (2 * g + (c >> 3)) * 2752 + p * 16 : 0x7fffffff - (1 << 24);
      const int soff = (128 * rq + 32 * w) * 2752 + c0 * 16;
#pragma unroll
      for (int i = 0; i < 4; ++i) { G[i] = ldnt(rg, voff, soff + i * 8 * 2752); SB(); } }
    { const int voff = g * 1024 + c * 16, soff = (128 * h + 64 * rh + 16 * w) * 1024 + cq * 256;
#pragma unroll
      for (int i = 0; i < 4; ++i) { O[i] = ldnt(ro, voff, soff + i * 4096); SB(); }
      const int so2 = (8 * c0 + rh * 4 * nch + 4 * w) * 1024 + cq * 256;
      Dn[0] = ldnt(rd, voff, so2); SB();
      Dn[1] = ldnt(rd, (w + 4 < nch) ? voff : 0x7fffffff - (1 << 24), so2 + 16 * 1024); SB(); }
    STAMP(1);
    lds_wait(sync + F_X);
    const unsigned tA = lds_ld(sync + W_TAG), tB = ~tA;
    const f32x4 st = *(LAS f32x4*)(L + L_STAT);
    { const float r0 = 1.0f / st.y, r1 = 1.0f / st.w;
      f32x4 a0 = {0.f, 0.f, 0.f, 0.f}, a1 = {0.f, 0.f, 0.f, 0.f};
#pragma unroll
      for (int i = 0; i < 4; ++i) {
        const int row = 128 * rq + 32 * w + 8 * i + 2 * g + (c >> 3);
        const unsigned raw0 = *(LAS u16*)(L + L_X + row * 2), raw1 = *(LAS u16*)(L + L_X + (512 + row) * 2);
        const float n0 = divr(rbf(bf2f(raw0) - st.x), st.y, r0), n1 = divr(rbf(bf2f(raw1) - st.z), st.w, r1);
        a0 = mfma(mask & cvtpk(n0, n0), G[i], a0);
        a1 = mfma(mask & cvtpk(n1, n1), G[i], a1);
      }
      if ((g >> 1) == (c >> 3) && (c & 7) < 6) {
        const int o = L_REDM + ((w * 2 + (c >> 3)) * 2 * 48 + 8 * (c & 7) + 4 * (g & 1)) * 4;
        *(LAS f32x4*)(L + o) = a0; *(LAS f32x4*)(L + o + 48 * 4) = a1;
      } }
    STAMP(2);
    if (lds_arrive(sync + C_M, lane) == 3u) {
      if (lane < 48) {
        f32x2 t = {0.f, 0.f};
#pragma unroll
        for (int k = 0; k < 8; ++k) t += *(LAS f32x2*)(L + L_REDM + k * 384 + lane * 8);
        stc(rw, WS_S1M + (h * 8 + j) * 1024 + lane * 16, t.x, t.y, tA, tB);
      }
    }
    STAMP(3);
    wait_ticks(DELAYM);
    unsigned hpk0, hpk1;
    { const int b = c >> 3, dq = (c >> 2) & 1; const int fl = rh * 4 * nch + 4 * (w + 4 * dq) + g;
      const bool act = (w + 4 * dq) < nch;
      const int go = WS_S1M + (h * 8 + 2 * (c & 3)) * 1024 + (b * 24 + (fl >> 1)) * 16;
      float gv, uv;
      _Pragma("unroll 1") for (unsigned n = 0; n < SPIN_CAP; ++n) {
        CBAR();
        const v4u pg = ldc(rw, go), pu = ldc(rw, go + 1024);
        const bool ok = !act || ((pg.y == tA) & (pg.w == tB) & (pu.y == tA) & (pu.w == tB));
        gv = __uint_as_float((g & 1) ? pg.z : pg.x); uv = __uint_as_float((g & 1) ? pu.z : pu.x);
        if (__all(ok)) break;
      }
      gv += dppf<XOR1>(gv); uv += dppf<XOR1>(uv); gv += dppf<XOR2>(gv); uv += dppf<XOR2>(uv);
      gv = rbf(gv); uv = rbf(uv);
      const float sg = rbf(1.0f / (1.0f + __builtin_amdgcn_exp2f(-gv * 1.4426950408889634f)));
      const float hv = act ? sg * uv : 0.f;
      const unsigned hp = cvtpk(hv, hv);
      hpk0 = (unsigned)__builtin_amdgcn_update_dpp((int)hp, (int)hp, 0x114, 0xf, 0xa, false);
      hpk1 = (unsigned)__builtin_amdgcn_update_dpp((int)hp, (int)hp, 0x104, 0xf, 0x5, false);
    }
    STAMP(4);
    f32x4 acc = {0.f, 0.f, 0.f, 0.f};
    acc = mfma(mask & hpk0, Dn[0], acc);
    acc = mfma(mask & hpk1, Dn[1], acc);
    {
      const int b = c >> 3, dim = 64 * rh + 16 * w + 4 * (c & 3) + g;
      const int go = WS_S2 + (h * 8 + b * 4) * 2048;
      wait_ticks(DELAY2);
      float ov[4], mv, lv;
      _Pragma("unroll 1") for (unsigned n = 0; n < SPIN_CAP; ++n) {
        CBAR();
        v4u p[4];
#pragma unroll
        for (int s = 0; s < 4; ++s) p[s] = ldc(rw, go + s * 2048 + (dim >> 1) * 16);
        const v4u pm = ldc(rw, go + (c & 3) * 2048 + 1024);
        bool ok = (pm.y == tA) & (pm.w == tB);
#pragma unroll
        for (int s = 0; s < 4; ++s) { ok &= (p[s].y == tA) & (p[s].w == tB); ov[s] = __uint_as_float((g & 1) ? p[s].z : p[s].x); }
        mv = __uint_as_float(pm.x); lv = __uint_as_float(pm.z);
        if (__all(ok)) break;
      }
      STAMP(5);
      float M = fmaxf(mv, dppf<XOR1>(mv)); M = fmaxf(M, dppf<XOR2>(M));
      const float e = __builtin_amdgcn_exp2f((mv - M) * 1.4426950408889634f);
      float Ls = e * lv; Ls += dppf<XOR1>(Ls); Ls += dppf<XOR2>(Ls);
      const float wt = e / Ls;
      float o = dppf<0x00>(wt) * ov[0];
      o = fmaf(dppf<0x55>(wt), ov[1], o); o = fmaf(dppf<0xAA>(wt), ov[2], o); o = fmaf(dppf<0xFF>(wt), ov[3], o);
      const unsigned opk = cvtpk(o, o);
      acc = mfma(mask & dppu<0x00>(opk), O[0], acc);
      acc = mfma(mask & dppu<0x55>(opk), O[1], acc);
      acc = mfma(mask & dppu<0xAA>(opk), O[2], acc);
      acc = mfma(mask & dppu<0xFF>(opk), O[3], acc);
    }
    *(LAS f32x4*)(L + L_RED2 + (w * 64 + lane) * 16) = acc;
    STAMP(6);
    if (lds_arrive(sync + C_2, lane) == 3u) {
      f32x4 t = *(LAS f32x4*)(L + L_RED2 + lane * 16);
#pragma unroll
      for (int k = 1; k < 4; ++k) t += *(LAS f32x4*)(L + L_RED2 + (k * 64 + lane) * 16);
      const int cg = (g >> 1) * 128 + 32 * cq + 2 * c + (g & 1);
      const int po = WS_S3 + cg * 2048 + (2 * h + rh) * 32;
      stc(rw, po, t.x, t.y, tA, tB); stc(rw, po + 16, t.z, t.w, tA, tB);
      CBAR();
      if (lane == 0) lds_st(sync + F_C, 1u);
      STAMP(7);
    }
  }
  STAMP_FLUSH();
}

extern "C" void kernel_launch(void* const* d_in, const int* in_sizes, int n_in, void* d_out, int out_size, void* d_ws, size_t ws_size, hipStream_t stream) {
  fused_decode<<<256, 1024, 0, stream>>>((const u16*)d_in[0], (const u16*)d_in[1], (const u16*)d_in[2], (const u16*)d_in[3], (const u16*)d_in[4],
                                         (const u16*)d_in[6], (const u16*)d_in[7], (const u16*)d_in[8], (u16*)d_out, (unsigned char*)d_ws);
}
```

```cpp
#define AMPWAVE 0
#define AMPSTAMP 0
#define AMPWG 77
#define AMPK 8
#include <hip/hip_runtime.h>
#include <stdint.h>

typedef unsigned short u16;
typedef unsigned v4u __attribute__((ext_vector_type(4)));
typedef unsigned v2u __attribute__((ext_vector_type(2)));
typedef float f32x4 __attribute__((ext_vector_type(4)));
typedef float f32x2 __attribute__((ext_vector_type(2)));
typedef short bf16x8 __attribute__((ext_vector_type(8)));
typedef __bf16 bf16x2 __attribute__((ext_vector_type(2)));
#define LAS __attribute__((address_space(3)))

#define DELAY1 60
#define DELAYM 40
#define DELAY2 0
#define DELAY3 30
#define CPRIO 0

namespace {
constexpr int AUX_NT = 2, AUX_SC1 = 16;
constexpr unsigned RSRC3 = 0x00020000u;
constexpr int WS_EPOCH = 0;
constexpr int WS_S1Q = 4096;
constexpr int WS_S1M = WS_S1Q + 256 * 2048;
constexpr int WS_S2 = WS_S1M + 256 * 1024;
constexpr int WS_S3 = WS_S2 + 256 * 2048;
constexpr int WS_END = WS_S3 + 256 * 2048;
constexpr int WS_STAMP = 8 << 20;
constexpr int L_SYNC = 0;
constexpr int L_STAT = 64;
constexpr int L_Q = 128;
constexpr int L_X = 512;
constexpr int L_RED1 = 2560;
constexpr int L_REDM = L_RED1 + 4096;
constexpr int L_RED2 = L_REDM + 3072;
constexpr int L_OB = L_RED2 + 4096;
constexpr int L_ML = L_OB + 4096;
constexpr int L_END = L_ML + 64;
enum { C_A = 0, C_B = 1, C_M = 2, C_2 = 3, F_Q = 4, F_X = 5, F_C = 6, W_TAG = 7 };
constexpr unsigned SPIN_CAP = 1u << 22;

__device__ __forceinline__ float bf2f(unsigned v) { return __uint_as_float(v << 16); }
__device__ __forceinline__ float bflo(unsigned v) { return __uint_as_float(v << 16); }
__device__ __forceinline__ float bfhi(unsigned v) { return __uint_as_float(v & 0xffff0000u); }
__device__ __forceinline__ unsigned cvtpk(float a, float b) { f32x2 v = {a, b}; return __builtin_bit_cast(unsigned, __builtin_convertvector(v, bf16x2)); }
__device__ __forceinline__ float rbf(float a) { return bflo(cvtpk(a, a)); }
template <int CTRL> __device__ __forceinline__ unsigned dppu(unsigned x) { return (unsigned)__builtin_amdgcn_mov_dpp((int)x, CTRL, 0xf, 0xf, true); }
template <int CTRL> __device__ __forceinline__ float dppf(float x) { return __uint_as_float(dppu<CTRL>(__float_as_uint(x))); }
constexpr int XOR1 = 0xB1, XOR2 = 0x4E, XOR7 = 0x141, MIR = 0x140, ROR4 = 0x124, ROR8 = 0x128, NEWB = 0x150;
__device__ __forceinline__ float x16_sum(float x) { auto s = __builtin_amdgcn_permlane16_swap(__float_as_uint(x), __float_as_uint(x), false, false); return __uint_as_float(s[0]) + __uint_as_float(s[1]); }
__device__ __forceinline__ float x32_sum(float x) { auto s = __builtin_amdgcn_permlane32_swap(__float_as_uint(x), __float_as_uint(x), false, false); return __uint_as_float(s[0]) + __uint_as_float(s[1]); }
__device__ __forceinline__ float x16_max(float x) { auto s = __builtin_amdgcn_permlane16_swap(__float_as_uint(x), __float_as_uint(x), false, false); return fmaxf(__uint_as_float(s[0]), __uint_as_float(s[1])); }
__device__ __forceinline__ float x32_max(float x) { auto s = __builtin_amdgcn_permlane32_swap(__float_as_uint(x), __float_as_uint(x), false, false); return fmaxf(__uint_as_float(s[0]), __uint_as_float(s[1])); }
__device__ __forceinline__ f32x4 mfma(v4u a, v4u b, f32x4 c) { return __builtin_amdgcn_mfma_f32_16x16x32_bf16(__builtin_bit_cast(bf16x8, a), __builtin_bit_cast(bf16x8, b), c, 0, 0, 0); }
__device__ __forceinline__ float dot2(unsigned a, unsigned b, float c) { return __builtin_amdgcn_fdot2_f32_bf16(__builtin_bit_cast(bf16x2, a), __builtin_bit_cast(bf16x2, b), c, false); }
__device__ __forceinline__ __amdgpu_buffer_rsrc_t rsrc(const void* p, unsigned bytes) { return __builtin_amdgcn_make_buffer_rsrc((void*)p, 0, (int)bytes, RSRC3); }
__device__ __forceinline__ v4u ldnt(__amdgpu_buffer_rsrc_t r, int voff, int soff) { return __builtin_amdgcn_raw_buffer_load_b128(r, voff, soff, AUX_NT); }
__device__ __forceinline__ v4u ldc(__amdgpu_buffer_rsrc_t r, int voff) { return __builtin_amdgcn_raw_buffer_load_b128(r, voff, 0, AUX_SC1); }
__device__ __forceinline__ void stc(__amdgpu_buffer_rsrc_t r, int voff, float a, float b, unsigned tA, unsigned tB) {
  v4u v = {__float_as_uint(a), tA, __float_as_uint(b), tB};
  __builtin_amdgcn_raw_buffer_store_b128(v, r, voff, 0, AUX_SC1);
}
#define CBAR() asm volatile("" ::: "memory")
#define SB() __builtin_amdgcn_sched_barrier(0)
__device__ __forceinline__ unsigned long long rt() { return __builtin_amdgcn_s_memrealtime(); }
__device__ __forceinline__ void wait_ticks(int n) { if (n > 0) { const unsigned long long t = rt() + n; while (rt() < t) __builtin_amdgcn_s_sleep(1); } }
__device__ __forceinline__ unsigned lds_ld(LAS unsigned* p) { return __hip_atomic_load(p, __ATOMIC_RELAXED, __HIP_MEMORY_SCOPE_WORKGROUP); }
__device__ __forceinline__ void lds_st(LAS unsigned* p, unsigned v) { __hip_atomic_store(p, v, __ATOMIC_RELAXED, __HIP_MEMORY_SCOPE_WORKGROUP); }
__device__ __forceinline__ void lds_wait(LAS unsigned* p) {
  _Pragma("unroll 1") for (unsigned n = 0; n < SPIN_CAP; ++n) { CBAR(); if (lds_ld(p) != 0u) break; __builtin_amdgcn_s_sleep(1); }
  CBAR();
}
__device__ __forceinline__ unsigned lds_arrive(LAS unsigned* p, int lane) {
  CBAR();
  unsigned old = 0;
  if (lane == 0) old = __hip_atomic_fetch_add(p, 1u, __ATOMIC_RELAXED, __HIP_MEMORY_SCOPE_WORKGROUP);
  old = __builtin_amdgcn_readfirstlane(old);
  CBAR();
  return old;
}
__device__ __forceinline__ float divr(float x, float d, float r) { const float q = x * r; return fmaf(fmaf(-q, d, x), r, q); }
#define STAMP(k) do { SB(); ts[k] = rt(); SB(); } while (0)
#define STAMP_DECL unsigned long long ts[10] = {0, 0, 0, 0, 0, 0, 0, 0, 0, 0}
#define STAMP_FLUSH() do { if (blockIdx.x == AMPWG && wave == AMPWAVE) { const unsigned long long e_ = rt() + AMPK * (ts[AMPSTAMP] - ts[0]); while (rt() < e_) __builtin_amdgcn_s_sleep(4); } } while (0)
}

__global__ void __launch_bounds__(1024, 4)
fused_decode(const u16* __restrict__ x, const u16* __restrict__ kc, const u16* __restrict__ vc, const u16* __restrict__ wq,
             const u16* __restrict__ wo, const u16* __restrict__ wg, const u16* __restrict__ wu, const u16* __restrict__ wd,
             u16* __restrict__ out, unsigned char* __restrict__ ws) {
  __shared__ __attribute__((aligned(16))) unsigned char smem[L_END];
  LAS unsigned char* L = (LAS unsigned char*)smem;
  LAS unsigned* sync = (LAS unsigned*)(L + L_SYNC);
  const int tid = threadIdx.x, lane = tid & 63, wave = __builtin_amdgcn_readfirstlane(tid >> 6);
  const int wgid = blockIdx.x, jj = wgid >> 3, h = (wgid & 7) * 4 + (jj >> 3), j = jj & 7;
  const int g = lane >> 4, c = lane & 15;
  STAMP_DECL;
  STAMP(0);
  asm volatile("" :: "s"(x), "s"(kc), "s"(vc), "s"(wq), "s"(wo), "s"(wg), "s"(wu), "s"(wd), "s"(out), "s"(ws));
  v4u mask;
  { const unsigned half = (c & 1) ? 0xffff0000u : 0x0000ffffu; const int kk = (c & 7) >> 1;
    mask.x = kk == 0 ? half : 0u; mask.y = kk == 1 ? half : 0u; mask.z = kk == 2 ? half : 0u; mask.w = kk == 3 ? half : 0u; }
  const __amdgpu_buffer_rsrc_t rw = rsrc(ws, WS_END);
  const int h7 = h & 7, nch = 5 + ((h7 == 0 || h7 == 1 || h7 == 4) ? 1 : 0);
  const int c0 = 5 * h + 3 * (h >> 3) + (h7 > 0) + (h7 > 1) + (h7 > 4);

  if (wave < 4) {
    const int a = wave;
    if (a == 0) {
      if (lane < 16) sync[lane] = 0u;
    }
    const __amdgpu_buffer_rsrc_t rx = rsrc(x, 2048), rq = rsrc(wq, 512 * 4096 * 2);
    const v4u x0 = __builtin_amdgcn_raw_buffer_load_b128(rx, lane * 32, 0, 0), x1 = __builtin_amdgcn_raw_buffer_load_b128(rx, lane * 32 + 16, 0, 0);
    SB();
    unsigned xo = 0, ep = 0;
    if (a == 0) { xo = __builtin_amdgcn_raw_buffer_load_b32(rx, wgid * 8 + (lane & 1) * 4, 0, 0); ep = __builtin_amdgcn_raw_buffer_load_b32(rw, WS_EPOCH, 0, 0); }
    SB();
    v4u w[4];
    { const int voff = g * 8192 + c * 16, soff = (64 * j + 16 * a) * 8192 + h * 256;
#pragma unroll
      for (int i = 0; i < 4; ++i) { w[i] = ldnt(rq, voff, soff + i * 4 * 8192); SB(); } }
    asm volatile("s_waitcnt lgkmcnt(0)" ::: "memory");
    __builtin_amdgcn_s_barrier();
    CBAR();
    STAMP(1);
    unsigned tA, tB;
    *(LAS v4u*)(L + L_X + lane * 32) = x0; *(LAS v4u*)(L + L_X + lane * 32 + 16) = x1;
    float xv[16];
    { const unsigned r[8] = {x0.x, x0.y, x0.z, x0.w, x1.x, x1.y, x1.z, x1.w};
#pragma unroll
      for (int i = 0; i < 8; ++i) { xv[2 * i] = bflo(r[i]); xv[2 * i + 1] = bfhi(r[i]); } }
    float s = 0.f;
#pragma unroll
    for (int i = 0; i < 16; i += 4) s += (xv[i] + xv[i + 1]) + (xv[i + 2] + xv[i + 3]);
    s += dppf<XOR1>(s); s += dppf<XOR2>(s); s += dppf<XOR7>(s); s += dppf<MIR>(s); s = x16_sum(s);
    const float mean = s * (1.0f / 512.0f);
    float v = 0.f;
#pragma unroll
    for (int i = 0; i < 16; ++i) { const float d = xv[i] - mean; v = fmaf(d, d, v); }
    v += dppf<XOR1>(v); v += dppf<XOR2>(v); v += dppf<XOR7>(v); v += dppf<MIR>(v); v = x16_sum(v);
    const float mb = rbf(mean), den = rbf(sqrtf(rbf(rbf(v * (1.0f / 512.0f)) + 1e-5f)));
    const float m0 = __uint_as_float(__builtin_amdgcn_readlane(__float_as_uint(mb), 0)), m1 = __uint_as_float(__builtin_amdgcn_readlane(__float_as_uint(mb), 32));
    const float d0 = __uint_as_float(__builtin_amdgcn_readlane(__float_as_uint(den), 0)), d1 = __uint_as_float(__builtin_amdgcn_readlane(__float_as_uint(den), 32));
    if (a == 0) {
      asm volatile("" : "+v"(ep));
      tA = ep + 1u;
      if (lane == 0) { *(LAS f32x4*)(L + L_STAT) = f32x4{m0, d0, m1, d1}; lds_st(sync + W_TAG, tA); CBAR(); lds_st(sync + F_X, 1u); }
    }
    STAMP(2);
    {
      const int b = c >> 3; const float mm = b ? m1 : m0, dd = b ? d1 : d0, rr = 1.0f / dd;
      f32x4 acc = {0.f, 0.f, 0.f, 0.f};
#pragma unroll
      for (int i = 0; i < 4; ++i) {
        const unsigned raw = *(LAS u16*)(L + L_X + (b * 512 + 64 * j + 16 * a + 4 * i + g) * 2);
        const float xn = divr(rbf(bf2f(raw) - mm), dd, rr);
        const unsigned pk = cvtpk(xn, xn);
        acc = mfma(mask & pk, w[i], acc);
      }
      *(LAS f32x4*)(L + L_RED1 + (a * 64 + lane) * 16) = acc;
    }
    STAMP(3);
    if (lds_arrive(sync + C_A, lane) == 3u) {
      tA = lds_ld(sync + W_TAG); tB = ~tA;
      f32x4 t = *(LAS f32x4*)(L + L_RED1 + lane * 16);
#pragma unroll
      for (int k = 1; k < 4; ++k) t += *(LAS f32x4*)(L + L_RED1 + (k * 64 + lane) * 16);
      const int po = WS_S1Q + (h * 8 + j) * 2048 + ((g >> 1) * 64 + 4 * c + 2 * (g & 1)) * 16;
      stc(rw, po, t.x, t.y, tA, tB); stc(rw, po + 16, t.z, t.w, tA, tB);
      STAMP(4);
      wait_ticks(DELAY1);
      const int go = WS_S1Q + h * 8 * 2048 + ((j >> 2) * 64 + lane) * 16;
      float q0, q1;
      _Pragma("unroll 1") for (unsigned n = 0; n < SPIN_CAP; ++n) {
        CBAR();
        v4u p[8];
#pragma unroll
        for (int k = 0; k < 8; ++k) p[k] = ldc(rw, go + k * 2048);
        bool ok = true; q0 = 0.f; q1 = 0.f;
#pragma unroll
        for (int k = 0; k < 8; ++k) { ok &= (p[k].y == tA) & (p[k].w == tB); q0 += __uint_as_float(p[k].x); q1 += __uint_as_float(p[k].z); }
        if (__all(ok)) break;
      }
      *(LAS unsigned*)(L + L_Q + lane * 4) = cvtpk(q0, q1);
      CBAR();
      if (lane == 0) lds_st(sync + F_Q, 1u);
      STAMP(5);
    }
    if (a == 0) {
      lds_wait(sync + F_C);
      STAMP(6);
      tA = ep + 1u; tB = ~tA;
      wait_ticks(DELAY3);
      float y0, y1;
      _Pragma("unroll 1") for (unsigned n = 0; n < SPIN_CAP; ++n) {
        CBAR();
        const v4u p0 = ldc(rw, WS_S3 + wgid * 2048 + lane * 16), p1 = ldc(rw, WS_S3 + wgid * 2048 + 1024 + lane * 16);
        const bool ok = (p0.y == tA) & (p0.w == tB) & (p1.y == tA) & (p1.w == tB);
        y0 = __uint_as_float(p0.x) + __uint_as_float(p1.x); y1 = __uint_as_float(p0.z) + __uint_as_float(p1.z);
        if (__all(ok)) break;
      }
      STAMP(7);
      y0 += dppf<XOR2>(y0); y1 += dppf<XOR2>(y1);
      y0 += dppf<ROR4>(y0); y1 += dppf<ROR4>(y1);
      y0 += dppf<ROR8>(y0); y1 += dppf<ROR8>(y1);
      y0 = x32_sum(x16_sum(y0)); y1 = x32_sum(x16_sum(y1));
      if (lane < 2) ((unsigned*)out)[wgid * 2 + lane] = cvtpk(bflo(xo) + y0, bfhi(xo) + y1);
      if (wgid == 0 && lane == 0) __builtin_amdgcn_raw_buffer_store_b32(tA, rw, WS_EPOCH, 0, AUX_SC1);
      STAMP(8);
    }
  } else if (wave < 12) {
    const int bw = wave - 4;
    __builtin_amdgcn_s_barrier();
    CBAR();
    const __amdgpu_buffer_rsrc_t rk = rsrc(kc, 2u * 32 * 4096 * 128 * 2), rv = rsrc(vc, 2u * 32 * 4096 * 128 * 2);
    const int soff = (((j >> 2) * 32 + h) * 4096 + 3072 + (j & 3) * 256 + bw * 32) * 256;
    v4u K[8], V[8];
#pragma unroll
    for (int i = 0; i < 8; ++i) { K[i] = ldnt(rk, lane * 16 + (i & 3) * 1024, soff + (i >> 2) * 4096); SB(); }
#pragma unroll
    for (int i = 0; i < 8; ++i) { V[i] = ldnt(rv, lane * 16 + (i & 3) * 1024, soff + (i >> 2) * 4096); SB(); }
    STAMP(1);
    lds_wait(sync + F_Q);
    STAMP(2);
    const v4u q = *(LAS v4u*)(L + L_Q + c * 16);
    float sc[8];
#pragma unroll
    for (int i = 0; i < 8; ++i) sc[i] = dot2(K[i].w, q.w, dot2(K[i].z, q.z, dot2(K[i].y, q.y, dot2(K[i].x, q.x, 0.f))));
    { const bool b3 = c & 8, b2 = c & 4, b1 = c & 2;
#pragma unroll
      for (int i = 0; i < 4; ++i) { const float keep = b3 ? sc[i + 4] : sc[i], send = b3 ? sc[i] : sc[i + 4]; sc[i] = keep + dppf<ROR8>(send); }
#pragma unroll
      for (int i = 0; i < 2; ++i) { const float keep = b2 ? sc[i + 2] : sc[i], send = b2 ? sc[i] : sc[i + 2]; sc[i] = keep + dppf<XOR7>(send); }
      { const float keep = b1 ? sc[1] : sc[0], send = b1 ? sc[0] : sc[1]; sc[0] = keep + dppf<XOR2>(send); }
      sc[0] += dppf<XOR1>(sc[0]); }
    const float sv = rbf(rbf(sc[0]) * 0.08838834764831845f);
    float m = fmaxf(sv, dppf<XOR2>(sv)); m = fmaxf(m, dppf<ROR4>(m)); m = fmaxf(m, dppf<ROR8>(m)); m = x32_max(x16_max(m));
    const float p = __builtin_amdgcn_exp2f((sv - m) * 1.4426950408889634f);
    float l = p + dppf<XOR2>(p); l += dppf<ROR4>(l); l += dppf<ROR8>(l); l = x32_sum(x16_sum(l));
    const unsigned ppk = cvtpk(p, p);
    const v4u mk = (c & 8) ? v4u{0u, 0u, 0u, 0u} : mask;
    STAMP(3);
    f32x4 acc = {0.f, 0.f, 0.f, 0.f};
#define PVSTEP(i) acc = mfma(mk & dppu<NEWB + 2 * (i)>(ppk), V[i], acc);
    PVSTEP(0) PVSTEP(1) PVSTEP(2) PVSTEP(3) PVSTEP(4) PVSTEP(5) PVSTEP(6) PVSTEP(7)
#undef PVSTEP
    if (lane < 32) *(LAS f32x4*)(L + L_OB + bw * 512 + (8 * c + 4 * g) * 4) = acc;
    if (lane == 0) *(LAS f32x2*)(L + L_ML + bw * 8) = f32x2{m, l};
    STAMP(4);
    if (lds_arrive(sync + C_B, lane) == 7u) {
      const unsigned tA = lds_ld(sync + W_TAG), tB = ~tA;
      f32x2 ml[8], o[8];
#pragma unroll
      for (int k = 0; k < 8; ++k) { ml[k] = *(LAS f32x2*)(L + L_ML + k * 8); o[k] = *(LAS f32x2*)(L + L_OB + k * 512 + lane * 8); }
      float M = ml[0].x;
#pragma unroll
      for (int k = 1; k < 8; ++k) M = fmaxf(M, ml[k].x);
      float Ls = 0.f, o0 = 0.f, o1 = 0.f;
#pragma unroll
      for (int k = 0; k < 8; ++k) { const float e = __builtin_amdgcn_exp2f((ml[k].x - M) * 1.4426950408889634f); Ls = fmaf(e, ml[k].y, Ls); o0 = fmaf(e, o[k].x, o0); o1 = fmaf(e, o[k].y, o1); }
      const int po = WS_S2 + (h * 8 + j) * 2048;
      stc(rw, po + lane * 16, o0, o1, tA, tB);
      if (lane == 0) stc(rw, po + 1024, M, Ls, tA, tB);
      STAMP(5);
    }
  } else {
    const int w = wave - 12;
    __builtin_amdgcn_s_barrier();
    CBAR();
    const int mm_ = j & 1, rq = j >> 1;
    const int cq = j & 3, rh = j >> 2;
    const __amdgpu_buffer_rsrc_t rg = rsrc(mm_ ? wu : wg, 512 * 1376 * 2), ro = rsrc(wo, 4096 * 512 * 2), rd = rsrc(wd, 1376 * 512 * 2);
    v4u G[4], O[4], Dn[2];
    { const int p = c & 7; const int voff = p < nch ? (2 * g + (c >> 3)) * 2752 + p * 16 : 0x7fffffff - (1 << 24);
      const int soff = (128 * rq + 32 * w) * 2752 + c0 * 16;
#pragma unroll
      for (int i = 0; i < 4; ++i) { G[i] = ldnt(rg, voff, soff + i * 8 * 2752); SB(); } }
    { const int voff = g * 1024 + c * 16, soff = (128 * h + 64 * rh + 16 * w) * 1024 + cq * 256;
#pragma unroll
      for (int i = 0; i < 4; ++i) { O[i] = ldnt(ro, voff, soff + i * 4096); SB(); }
      const int so2 = (8 * c0 + rh * 4 * nch + 4 * w) * 1024 + cq * 256;
      Dn[0] = ldnt(rd, voff, so2); SB();
      Dn[1] = ldnt(rd, (w + 4 < nch) ? voff : 0x7fffffff - (1 << 24), so2 + 16 * 1024); SB(); }
    STAMP(1);
    lds_wait(sync + F_X);
    const unsigned tA = lds_ld(sync + W_TAG), tB = ~tA;
    const f32x4 st = *(LAS f32x4*)(L + L_STAT);
    { const float r0 = 1.0f / st.y, r1 = 1.0f / st.w;
      f32x4 a0 = {0.f, 0.f, 0.f, 0.f}, a1 = {0.f, 0.f, 0.f, 0.f};
#pragma unroll
      for (int i = 0; i < 4; ++i) {
        const int row = 128 * rq + 32 * w + 8 * i + 2 * g + (c >> 3);
        const unsigned raw0 = *(LAS u16*)(L + L_X + row * 2), raw1 = *(LAS u16*)(L + L_X + (512 + row) * 2);
        const float n0 = divr(rbf(bf2f(raw0) - st.x), st.y, r0), n1 = divr(rbf(bf2f(raw1) - st.z), st.w, r1);
        a0 = mfma(mask & cvtpk(n0, n0), G[i], a0);
        a1 = mfma(mask & cvtpk(n1, n1), G[i], a1);
      }
      if ((g >> 1) == (c >> 3) && (c & 7) < 6) {
        const int o = L_REDM + ((w * 2 + (c >> 3)) * 2 * 48 + 8 * (c & 7) + 4 * (g & 1)) * 4;
        *(LAS f32x4*)(L + o) = a0; *(LAS f32x4*)(L + o + 48 * 4) = a1;
      } }
    STAMP(2);
    if (lds_arrive(sync + C_M, lane) == 3u) {
      if (lane < 48) {
        f32x2 t = {0.f, 0.f};
#pragma unroll
        for (int k = 0; k < 8; ++k) t += *(LAS f32x2*)(L + L_REDM + k * 384 + lane * 8);
        stc(rw, WS_S1M + (h * 8 + j) * 1024 + lane * 16, t.x, t.y, tA, tB);
      }
    }
    STAMP(3);
    wait_ticks(DELAYM);
    unsigned hpk0, hpk1;
    { const int b = c >> 3, dq = (c >> 2) & 1; const int fl = rh * 4 * nch + 4 * (w + 4 * dq) + g;
      const bool act = (w + 4 * dq) < nch;
      const int go = WS_S1M + (h * 8 + 2 * (c & 3)) * 1024 + (b * 24 + (fl >> 1)) * 16;
      float gv, uv;
      _Pragma("unroll 1") for (unsigned n = 0; n < SPIN_CAP; ++n) {
        CBAR();
        const v4u pg = ldc(rw, go), pu = ldc(rw, go + 1024);
        const bool ok = !act || ((pg.y == tA) & (pg.w == tB) & (pu.y == tA) & (pu.w == tB));
        gv = __uint_as_float((g & 1) ? pg.z : pg.x); uv = __uint_as_float((g & 1) ? pu.z : pu.x);
        if (__all(ok)) break;
      }
      gv += dppf<XOR1>(gv); uv += dppf<XOR1>(uv); gv += dppf<XOR2>(gv); uv += dppf<XOR2>(uv);
      gv = rbf(gv); uv = rbf(uv);
      const float sg = rbf(1.0f / (1.0f + __builtin_amdgcn_exp2f(-gv * 1.4426950408889634f)));
      const float hv = act ? sg * uv : 0.f;
      const unsigned hp = cvtpk(hv, hv);
      hpk0 = (unsigned)__builtin_amdgcn_update_dpp((int)hp, (int)hp, 0x114, 0xf, 0xa, false);
      hpk1 = (unsigned)__builtin_amdgcn_update_dpp((int)hp, (int)hp, 0x104, 0xf, 0x5, false);
    }
    STAMP(4);
    f32x4 acc = {0.f, 0.f, 0.f, 0.f};
    acc = mfma(mask & hpk0, Dn[0], acc);
    acc = mfma(mask & hpk1, Dn[1], acc);
    {
      const int b = c >> 3, dim = 64 * rh + 16 * w + 4 * (c & 3) + g;
      const int go = WS_S2 + (h * 8 + b * 4) * 2048;
      wait_ticks(DELAY2);
      float ov[4], mv, lv;
      _Pragma("unroll 1") for (unsigned n = 0; n < SPIN_CAP; ++n) {
        CBAR();
        v4u p[4];
#pragma unroll
        for (int s = 0; s < 4; ++s) p[s] = ldc(rw, go + s * 2048 + (dim >> 1) * 16);
        const v4u pm = ldc(rw, go + (c & 3) * 2048 + 1024);
        bool ok = (pm.y == tA) & (pm.w == tB);
#pragma unroll
        for (int s = 0; s < 4; ++s) { ok &= (p[s].y == tA) & (p[s].w == tB); ov[s] = __uint_as_float((g & 1) ? p[s].z : p[s].x); }
        mv = __uint_as_float(pm.x); lv = __uint_as_float(pm.z);
        if (__all(ok)) break;
      }
      STAMP(5);
      float M = fmaxf(mv, dppf<XOR1>(mv)); M = fmaxf(M, dppf<XOR2>(M));
      const float e = __builtin_amdgcn_exp2f((mv - M) * 1.4426950408889634f);
      float Ls = e * lv; Ls += dppf<XOR1>(Ls); Ls += dppf<XOR2>(Ls);
      const float wt = e / Ls;
      float o = dppf<0x00>(wt) * ov[0];
      o = fmaf(dppf<0x55>(wt), ov[1], o); o = fmaf(dppf<0xAA>(wt), ov[2], o); o = fmaf(dppf<0xFF>(wt), ov[3], o);
      const unsigned opk = cvtpk(o, o);
      acc = mfma(mask & dppu<0x00>(opk), O[0], acc);
      acc = mfma(mask & dppu<0x55>(opk), O[1], acc);
      acc = mfma(mask & dppu<0xAA>(opk), O[2], acc);
      acc = mfma(mask & dppu<0xFF>(opk), O[3], acc);
    }
    *(LAS f32x4*)(L + L_RED2 + (w * 64 + lane) * 16) = acc;
    STAMP(6);
    if (lds_arrive(sync + C_2, lane) == 3u) {
      f32x4 t = *(LAS f32x4*)(L + L_RED2 + lane * 16);
#pragma unroll
      for (int k = 1; k < 4; ++k) t += *(LAS f32x4*)(L + L_RED2 + (k * 64 + lane) * 16);
      const int cg = (g >> 1) * 128 + 32 * cq + 2 * c + (g & 1);
      const int po = WS_S3 + cg * 2048 + (2 * h + rh) * 32;
      stc(rw, po, t.x, t.y, tA, tB); stc(rw, po + 16, t.z, t.w, tA, tB);
      CBAR();
      if (lane == 0) lds_st(sync + F_C, 1u);
      STAMP(7);
    }
  }
  STAMP_FLUSH();
}

extern "C" void kernel_launch(void* const* d_in, const int* in_sizes, int n_in, void* d_out, int out_size, void* d_ws, size_t ws_size, hipStream_t stream) {
  fused_decode<<<256, 1024, 0, stream>>>((const u16*)d_in[0], (const u16*)d_in[1], (const u16*)d_in[2], (const u16*)d_in[3], (const u16*)d_in[4],
                                         (const u16*)d_in[6], (const u16*)d_in[7], (const u16*)d_in[8], (u16*)d_out, (unsigned char*)d_ws);
}
```

```cpp
#include <hip/hip_runtime.h>
#include <stdint.h>

typedef unsigned short u16;
typedef unsigned v4u __attribute__((ext_vector_type(4)));
typedef unsigned v2u __attribute__((ext_vector_type(2)));
typedef float f32x4 __attribute__((ext_vector_type(4)));
typedef float f32x2 __attribute__((ext_vector_type(2)));
typedef short bf16x8 __attribute__((ext_vector_type(8)));
typedef __bf16 bf16x2 __attribute__((ext_vector_type(2)));
#define LAS __attribute__((address_space(3)))

#define DELAY1 60
#define DELAYM 40
#define DELAY2 0
#define DELAY3 30
#define CPRIO 0

namespace {
constexpr int AUX_NT = 2, AUX_SC1 = 16;
constexpr unsigned RSRC3 = 0x00020000u;
constexpr int WS_EPOCH = 0;
constexpr int WS_S1Q = 4096;
constexpr int WS_S1M = WS_S1Q + 256 * 2048;
constexpr int WS_S2 = WS_S1M + 256 * 1024;
constexpr int WS_S3 = WS_S2 + 256 * 2048;
constexpr int WS_END = WS_S3 + 256 * 2048;
constexpr int WS_STAMP = 8 << 20;
constexpr int L_SYNC = 0;
constexpr int L_STAT = 64;
constexpr int L_Q = 128;
constexpr int L_X = 512;
constexpr int L_RED1 = 2560;
constexpr int L_REDM = L_RED1 + 4096;
constexpr int L_RED2 = L_REDM + 3072;
constexpr int L_OB = L_RED2 + 4096;
constexpr int L_ML = L_OB + 4096;
constexpr int L_END = L_ML + 64;
enum { C_A = 0, C_B = 1, C_M = 2, C_2 = 3, F_Q = 4, F_X = 5, F_C = 6, W_TAG = 7 };
constexpr unsigned SPIN_CAP = 1u << 22;

__device__ __forceinline__ float bf2f(unsigned v) { return __uint_as_float(v << 16); }
__device__ __forceinline__ float bflo(unsigned v) { return __uint_as_float(v << 16); }
__device__ __forceinline__ float bfhi(unsigned v) { return __uint_as_float(v & 0xffff0000u); }
__device__ __forceinline__ unsigned cvtpk(float a, float b) { f32x2 v = {a, b}; return __builtin_bit_cast(unsigned, __builtin_convertvector(v, bf16x2)); }
__device__ __forceinline__ float rbf(float a) { return bflo(cvtpk(a, a)); }
template <int CTRL> __device__ __forceinline__ unsigned dppu(unsigned x) { return (unsigned)__builtin_amdgcn_mov_dpp((int)x, CTRL, 0xf, 0xf, true); }
template <int CTRL> __device__ __forceinline__ float dppf(float x) { return __uint_as_float(dppu<CTRL>(__float_as_uint(x))); }
constexpr int XOR1 = 0xB1, XOR2 = 0x4E, XOR7 = 0x141, MIR = 0x140, ROR4 = 0x124, ROR8 = 0x128, NEWB = 0x150;
__device__ __forceinline__ float x16_sum(float x) { auto s = __builtin_amdgcn_permlane16_swap(__float_as_uint(x), __float_as_uint(x), false, false); return __uint_as_float(s[0]) + __uint_as_float(s[1]); }
__device__ __forceinline__ float x32_sum(float x) { auto s = __builtin_amdgcn_permlane32_swap(__float_as_uint(x), __float_as_uint(x), false, false); return __uint_as_float(s[0]) + __uint_as_float(s[1]); }
__device__ __forceinline__ float x16_max(float x) { auto s = __builtin_amdgcn_permlane16_swap(__float_as_uint(x), __float_as_uint(x), false, false); return fmaxf(__uint_as_float(s[0]), __uint_as_float(s[1])); }
__device__ __forceinline__ float x32_max(float x) { auto s = __builtin_amdgcn_permlane32_swap(__float_as_uint(x), __float_as_uint(x), false, false); return fmaxf(__uint_as_float(s[0]), __uint_as_float(s[1])); }
__device__ __forceinline__ f32x4 mfma(v4u a, v4u b, f32x4 c) { return __builtin_amdgcn_mfma_f32_16x16x32_bf16(__builtin_bit_cast(bf16x8, a), __builtin_bit_cast(bf16x8, b), c, 0, 0, 0); }
__device__ __forceinline__ float dot2(unsigned a, unsigned b, float c) { return __builtin_amdgcn_fdot2_f32_bf16(__builtin_bit_cast(bf16x2, a), __builtin_bit_cast(bf16x2, b), c, false); }
__device__ __forceinline__ __amdgpu_buffer_rsrc_t rsrc(const void* p, unsigned bytes) { return __builtin_amdgcn_make_buffer_rsrc((void*)p, 0, (int)bytes, RSRC3); }
__device__ __forceinline__ v4u ldnt(__amdgpu_buffer_rsrc_t r, int voff, int soff) { return __builtin_amdgcn_raw_buffer_load_b128(r, voff, soff, AUX_NT); }
__device__ __forceinline__ v4u ldc(__amdgpu_buffer_rsrc_t r, int voff) { return __builtin_amdgcn_raw_buffer_load_b128(r, voff, 0, AUX_SC1); }
__device__ __forceinline__ void stc(__amdgpu_buffer_rsrc_t r, int voff, float a, float b, unsigned tA, unsigned tB) {
  v4u v = {__float_as_uint(a), tA, __float_as_uint(b), tB};
  __builtin_amdgcn_raw_buffer_store_b128(v, r, voff, 0, AUX_SC1);
}
#define CBAR() asm volatile("" ::: "memory")
#define SB() __builtin_amdgcn_sched_barrier(0)
__device__ __forceinline__ unsigned long long rt() { return __builtin_amdgcn_s_memrealtime(); }
__device__ __forceinline__ void wait_ticks(int n) { if (n > 0) { const unsigned long long t = rt() + n; while (rt() < t) __builtin_amdgcn_s_sleep(1); } }
__device__ __forceinline__ unsigned lds_ld(LAS unsigned* p) { return __hip_atomic_load(p, __ATOMIC_RELAXED, __HIP_MEMORY_SCOPE_WORKGROUP); }
__device__ __forceinline__ void lds_st(LAS unsigned* p, unsigned v) { __hip_atomic_store(p, v, __ATOMIC_RELAXED, __HIP_MEMORY_SCOPE_WORKGROUP); }
__device__ __forceinline__ void lds_wait(LAS unsigned* p) {
  _Pragma("unroll 1") for (unsigned n = 0; n < SPIN_CAP; ++n) { CBAR(); if (lds_ld(p) != 0u) break; __builtin_amdgcn_s_sleep(1); }
  CBAR();
}
__device__ __forceinline__ unsigned lds_arrive(LAS unsigned* p, int lane) {
  CBAR();
  unsigned old = 0;
  if (lane == 0) old = __hip_atomic_fetch_add(p, 1u, __ATOMIC_RELAXED, __HIP_MEMORY_SCOPE_WORKGROUP);
  old = __builtin_amdgcn_readfirstlane(old);
  CBAR();
  return old;
}
__device__ __forceinline__ float divr(float x, float d, float r) { const float q = x * r; return fmaf(fmaf(-q, d, x), r, q); }
#define STAMP(k) do {} while (0)
#define STAMP_DECL do {} while (0)
#define STAMP_FLUSH() do {} while (0)
}

__global__ void __launch_bounds__(1024, 4)
fused_decode(const u16* __restrict__ x, const u16* __restrict__ kc, const u16* __restrict__ vc, const u16* __restrict__ wq,
             const u16* __restrict__ wo, const u16* __restrict__ wg, const u16* __restrict__ wu, const u16* __restrict__ wd,
             u16* __restrict__ out, unsigned char* __restrict__ ws) {
  __shared__ __attribute__((aligned(16))) unsigned char smem[L_END + 80 * 1024];
  LAS unsigned char* L = (LAS unsigned char*)smem;
  LAS unsigned* sync = (LAS unsigned*)(L + L_SYNC);
  const int tid = threadIdx.x, lane = tid & 63, wave = __builtin_amdgcn_readfirstlane(tid >> 6);
  const int wgid = blockIdx.x, jj = wgid >> 3, h = (wgid & 7) * 4 + (jj >> 3), j = jj & 7;
  const int g = lane >> 4, c = lane & 15;
  STAMP_DECL;
  STAMP(0);
  asm volatile("" :: "s"(x), "s"(kc), "s"(vc), "s"(wq), "s"(wo), "s"(wg), "s"(wu), "s"(wd), "s"(out), "s"(ws));
  v4u mask;
  { const unsigned half = (c & 1) ? 0xffff0000u : 0x0000ffffu; const int kk = (c & 7) >> 1;
    mask.x = kk == 0 ? half : 0u; mask.y = kk == 1 ? half : 0u; mask.z = kk == 2 ? half : 0u; mask.w = kk == 3 ? half : 0u; }
  const __amdgpu_buffer_rsrc_t rw = rsrc(ws, WS_END);
  const int h7 = h & 7, nch = 5 + ((h7 == 0 || h7 == 1 || h7 == 4) ? 1 : 0);
  const int c0 = 5 * h + 3 * (h >> 3) + (h7 > 0) + (h7 > 1) + (h7 > 4);

  if (wave < 4) {
    const int a = wave;
    if (a == 0) {
      if (lane < 16) sync[lane] = 0u;
    }
    const __amdgpu_buffer_rsrc_t rx = rsrc(x, 2048), rq = rsrc(wq, 512 * 4096 * 2);
    const v4u x0 = __builtin_amdgcn_raw_buffer_load_b128(rx, lane * 32, 0, 0), x1 = __builtin_amdgcn_raw_buffer_load_b128(rx, lane * 32 + 16, 0, 0);
    SB();
    unsigned xo = 0, ep = 0;
    if (a == 0) { xo = __builtin_amdgcn_raw_buffer_load_b32(rx, wgid * 8 + (lane & 1) * 4, 0, 0); ep = __builtin_amdgcn_raw_buffer_load_b32(rw, WS_EPOCH, 0, 0); }
    SB();
    v4u w[4];
    { const int voff = g * 8192 + c * 16, soff = (64 * j + 16 * a) * 8192 + h * 256;
#pragma unroll
      for (int i = 0; i < 4; ++i) { w[i] = ldnt(rq, voff, soff + i * 4 * 8192); SB(); } }
    asm volatile("s_waitcnt lgkmcnt(0)" ::: "memory");
    __builtin_amdgcn_s_barrier();
    CBAR();
    STAMP(1);
    unsigned tA, tB;
    *(LAS v4u*)(L + L_X + lane * 32) = x0; *(LAS v4u*)(L + L_X + lane * 32 + 16) = x1;
    float xv[16];
    { const unsigned r[8] = {x0.x, x0.y, x0.z, x0.w, x1.x, x1.y, x1.z, x1.w};
#pragma unroll
      for (int i = 0; i < 8; ++i) { xv[2 * i] = bflo(r[i]); xv[2 * i + 1] = bfhi(r[i]); } }
    float s = 0.f;
#pragma unroll
    for (int i = 0; i < 16; i += 4) s += (xv[i] + xv[i + 1]) + (xv[i + 2] + xv[i + 3]);
    s += dppf<XOR1>(s); s += dppf<XOR2>(s); s += dppf<XOR7>(s); s += dppf<MIR>(s); s = x16_sum(s);
    const float mean = s * (1.0f / 512.0f);
    float v = 0.f;
#pragma unroll
    for (int i = 0; i < 16; ++i) { const float d = xv[i] - mean; v = fmaf(d, d, v); }
    v += dppf<XOR1>(v); v += dppf<XOR2>(v); v += dppf<XOR7>(v); v += dppf<MIR>(v); v = x16_sum(v);
    const float mb = rbf(mean), den = rbf(sqrtf(rbf(rbf(v * (1.0f / 512.0f)) + 1e-5f)));
    const float m0 = __uint_as_float(__builtin_amdgcn_readlane(__float_as_uint(mb), 0)), m1 = __uint_as_float(__builtin_amdgcn_readlane(__float_as_uint(mb), 32));
    const float d0 = __uint_as_float(__builtin_amdgcn_readlane(__float_as_uint(den), 0)), d1 = __uint_as_float(__builtin_amdgcn_readlane(__float_as_uint(den), 32));
    if (a == 0) {
      asm volatile("" : "+v"(ep));
      tA = ep + 1u;
      if (lane == 0) { *(LAS f32x4*)(L + L_STAT) = f32x4{m0, d0, m1, d1}; lds_st(sync + W_TAG, tA); CBAR(); lds_st(sync + F_X, 1u); }
    }
    STAMP(2);
    {
      const int b = c >> 3; const float mm = b ? m1 : m0, dd = b ? d1 : d0, rr = 1.0f / dd;
      f32x4 acc = {0.f, 0.f, 0.f, 0.f};
#pragma unroll
      for (int i = 0; i < 4; ++i) {
        const unsigned raw = *(LAS u16*)(L + L_X + (b * 512 + 64 * j + 16 * a + 4 * i + g) * 2);
        const float xn = divr(rbf(bf2f(raw) - mm), dd, rr);
        const unsigned pk = cvtpk(xn, xn);
        acc = mfma(mask & pk, w[i], acc);
      }
      *(LAS f32x4*)(L + L_RED1 + (a * 64 + lane) * 16) = acc;
    }
    STAMP(3);
    if (lds_arrive(sync + C_A, lane) == 3u) {
      tA = lds_ld(sync + W_TAG); tB = ~tA;
      f32x4 t = *(LAS f32x4*)(L + L_RED1 + lane * 16);
#pragma unroll
      for (int k = 1; k < 4; ++k) t += *(LAS f32x4*)(L + L_RED1 + (k * 64 + lane) * 16);
      const int po = WS_S1Q + (h * 8 + j) * 2048 + ((g >> 1) * 64 + 4 * c + 2 * (g & 1)) * 16;
      stc(rw, po, t.x, t.y, tA, tB); stc(rw, po + 16, t.z, t.w, tA, tB);
      STAMP(4);
      wait_ticks(DELAY1);
      const int go = WS_S1Q + h * 8 * 2048 + ((j >> 2) * 64 + lane) * 16;
      float q0, q1;
      _Pragma("unroll 1") for (unsigned n = 0; n < SPIN_CAP; ++n) {
        CBAR();
        v4u p[8];
#pragma unroll
        for (int k = 0; k < 8; ++k) p[k] = ldc(rw, go + k * 2048);
        bool ok = true; q0 = 0.f; q1 = 0.f;
#pragma unroll
        for (int k = 0; k < 8; ++k) { ok &= (p[k].y == tA) & (p[k].w == tB); q0 += __uint_as_float(p[k].x); q1 += __uint_as_float(p[k].z); }
        if (__all(ok)) break;
      }
      *(LAS unsigned*)(L + L_Q + lane * 4) = cvtpk(q0, q1);
      CBAR();
      if (lane == 0) lds_st(sync + F_Q, 1u);
      STAMP(5);
    }
    if (a == 0) {
      lds_wait(sync + F_C);
      STAMP(6);
      tA = ep + 1u; tB = ~tA;
      wait_ticks(DELAY3);
      float y0, y1;
      _Pragma("unroll 1") for (unsigned n = 0; n < SPIN_CAP; ++n) {
        CBAR();
        const v4u p0 = ldc(rw, WS_S3 + wgid * 2048 + lane * 16), p1 = ldc(rw, WS_S3 + wgid * 2048 + 1024 + lane * 16);
        const bool ok = (p0.y == tA) & (p0.w == tB) & (p1.y == tA) & (p1.w == tB);
        y0 = __uint_as_float(p0.x) + __uint_as_float(p1.x); y1 = __uint_as_float(p0.z) + __uint_as_float(p1.z);
        if (__all(ok)) break;
      }
      STAMP(7);
      y0 += dppf<XOR2>(y0); y1 += dppf<XOR2>(y1);
      y0 += dppf<ROR4>(y0); y1 += dppf<ROR4>(y1);
      y0 += dppf<ROR8>(y0); y1 += dppf<ROR8>(y1);
      y0 = x32_sum(x16_sum(y0)); y1 = x32_sum(x16_sum(y1));
      if (lane < 2) ((unsigned*)out)[wgid * 2 + lane] = cvtpk(bflo(xo) + y0, bfhi(xo) + y1);
      if (wgid == 0 && lane == 0) __builtin_amdgcn_raw_buffer_store_b32(tA, rw, WS_EPOCH, 0, AUX_SC1);
      STAMP(8);
    }
  } else if (wave < 12) {
    const int bw = wave - 4;
    __builtin_amdgcn_s_barrier();
    CBAR();
    const __amdgpu_buffer_rsrc_t rk = rsrc(kc, 2u * 32 * 4096 * 128 * 2), rv = rsrc(vc, 2u * 32 * 4096 * 128 * 2);
    const int soff = (((j >> 2) * 32 + h) * 4096 + 3072 + (j & 3) * 256 + bw * 32) * 256;
    v4u K[8], V[8];
#pragma unroll
    for (int i = 0; i < 8; ++i) { K[i] = ldnt(rk, lane * 16 + (i & 3) * 1024, soff + (i >> 2) * 4096); SB(); }
#pragma unroll
    for (int i = 0; i < 8; ++i) { V[i] = ldnt(rv, lane * 16 + (i & 3) * 1024, soff + (i >> 2) * 4096); SB(); }
    STAMP(1);
    lds_wait(sync + F_Q);
    STAMP(2);
    const v4u q = *(LAS v4u*)(L + L_Q + c * 16);
    float sc[8];
#pragma unroll
    for (int i = 0; i < 8; ++i) sc[i] = dot2(K[i].w, q.w, dot2(K[i].z, q.z, dot2(K[i].y, q.y, dot2(K[i].x, q.x, 0.f))));
    { const bool b3 = c & 8, b2 = c & 4, b1 = c & 2;
#pragma unroll
      for (int i = 0; i < 4; ++i) { const float keep = b3 ? sc[i + 4] : sc[i], send = b3 ? sc[i] : sc[i + 4]; sc[i] = keep + dppf<ROR8>(send); }
#pragma unroll
      for (int i = 0; i < 2; ++i) { const float keep = b2 ? sc[i + 2] : sc[i], send = b2 ? sc[i] : sc[i + 2]; sc[i] = keep + dppf<XOR7>(send); }
      { const float keep = b1 ? sc[1] : sc[0], send = b1 ? sc[0] : sc[1]; sc[0] = keep + dppf<XOR2>(send); }
      sc[0] += dppf<XOR1>(sc[0]); }
    const float sv = rbf(rbf(sc[0]) * 0.08838834764831845f);
    float m = fmaxf(sv, dppf<XOR2>(sv)); m = fmaxf(m, dppf<ROR4>(m)); m = fmaxf(m, dppf<ROR8>(m)); m = x32_max(x16_max(m));
    const float p = __builtin_amdgcn_exp2f((sv - m) * 1.4426950408889634f);
    float l = p + dppf<XOR2>(p); l += dppf<ROR4>(l); l += dppf<ROR8>(l); l = x32_sum(x16_sum(l));
    const unsigned ppk = cvtpk(p, p);
    const v4u mk = (c & 8) ? v4u{0u, 0u, 0u, 0u} : mask;
    STAMP(3);
    f32x4 acc = {0.f, 0.f, 0.f, 0.f};
#define PVSTEP(i) acc = mfma(mk & dppu<NEWB + 2 * (i)>(ppk), V[i], acc);
    PVSTEP(0) PVSTEP(1) PVSTEP(2) PVSTEP(3) PVSTEP(4) PVSTEP(5) PVSTEP(6) PVSTEP(7)
#undef PVSTEP
    if (lane < 32) *(LAS f32x4*)(L + L_OB + bw * 512 + (8 * c + 4 * g) * 4) = acc;
    if (lane == 0) *(LAS f32x2*)(L + L_ML + bw * 8) = f32x2{m, l};
    STAMP(4);
    if (lds_arrive(sync + C_B, lane) == 7u) {
      const unsigned tA = lds_ld(sync + W_TAG), tB = ~tA;
      f32x2 ml[8], o[8];
#pragma unroll
      for (int k = 0; k < 8; ++k) { ml[k] = *(LAS f32x2*)(L + L_ML + k * 8); o[k] = *(LAS f32x2*)(L + L_OB + k * 512 + lane * 8); }
      float M = ml[0].x;
#pragma unroll
      for (int k = 1; k < 8; ++k) M = fmaxf(M, ml[k].x);
      float Ls = 0.f, o0 = 0.f, o1 = 0.f;
#pragma unroll
      for (int k = 0; k < 8; ++k) { const float e = __builtin_amdgcn_exp2f((ml[k].x - M) * 1.4426950408889634f); Ls = fmaf(e, ml[k].y, Ls); o0 = fmaf(e, o[k].x, o0); o1 = fmaf(e, o[k].y, o1); }
      const int po = WS_S2 + (h * 8 + j) * 2048;
      stc(rw, po + lane * 16, o0, o1, tA, tB);
      if (lane == 0) stc(rw, po + 1024, M, Ls, tA, tB);
      STAMP(5);
    }
  } else {
    const int w = wave - 12;
    __builtin_amdgcn_s_barrier();
    CBAR();
    const int mm_ = j & 1, rq = j >> 1;
    const int cq = j & 3, rh = j >> 2;
    const __amdgpu_buffer_rsrc_t rg = rsrc(mm_ ? wu : wg, 512 * 1376 * 2), ro = rsrc(wo, 4096 * 512 * 2), rd = rsrc(wd, 1376 * 512 * 2);
    v4u G[4], O[4], Dn[2];
    { const int p = c & 7; const int voff = p < nch ? (2 * g + (c >> 3)) * 2752 + p * 16 : 0x7fffffff - (1 << 24);
      const int soff = (128 * rq + 32 * w) * 2752 + c0 * 16;
#pragma unroll
      for (int i = 0; i < 4; ++i) { G[i] = ldnt(rg, voff, soff + i * 8 * 2752); SB(); } }
    { const int voff = g * 1024 + c * 16, soff = (128 * h + 64 * rh + 16 * w) * 1024 + cq * 256;
#pragma unroll
      for (int i = 0; i < 4; ++i) { O[i] = ldnt(ro, voff, soff + i * 4096); SB(); }
      const int so2 = (8 * c0 + rh * 4 * nch + 4 * w) * 1024 + cq * 256;
      Dn[0] = ldnt(rd, voff, so2); SB();
      Dn[1] = ldnt(rd, (w + 4 < nch) ? voff : 0x7fffffff - (1 << 24), so2 + 16 * 1024); SB(); }
    STAMP(1);
    lds_wait(sync + F_X);
    const unsigned tA = lds_ld(sync + W_TAG), tB = ~tA;
    const f32x4 st = *(LAS f32x4*)(L + L_STAT);
    { const float r0 = 1.0f / st.y, r1 = 1.0f / st.w;
      f32x4 a0 = {0.f, 0.f, 0.f, 0.f}, a1 = {0.f, 0.f, 0.f, 0.f};
#pragma unroll
      for (int i = 0; i < 4; ++i) {
        const int row = 128 * rq + 32 * w + 8 * i + 2 * g + (c >> 3);
        const unsigned raw0 = *(LAS u16*)(L + L_X + row * 2), raw1 = *(LAS u16*)(L + L_X + (512 + row) * 2);
        const float n0 = divr(rbf(bf2f(raw0) - st.x), st.y, r0), n1 = divr(rbf(bf2f(raw1) - st.z), st.w, r1);
        a0 = mfma(mask & cvtpk(n0, n0), G[i], a0);
        a1 = mfma(mask & cvtpk(n1, n1), G[i], a1);
      }
      if ((g >> 1) == (c >> 3) && (c & 7) < 6) {
        const int o = L_REDM + ((w * 2 + (c >> 3)) * 2 * 48 + 8 * (c & 7) + 4 * (g & 1)) * 4;
        *(LAS f32x4*)(L + o) = a0; *(LAS f32x4*)(L + o + 48 * 4) = a1;
      } }
    STAMP(2);
    if (lds_arrive(sync + C_M, lane) == 3u) {
      if (lane < 48) {
        f32x2 t = {0.f, 0.f};
#pragma unroll
        for (int k = 0; k < 8; ++k) t += *(LAS f32x2*)(L + L_REDM + k * 384 + lane * 8);
        stc(rw, WS_S1M + (h * 8 + j) * 1024 + lane * 16, t.x, t.y, tA, tB);
      }
    }
    STAMP(3);
    wait_ticks(DELAYM);
    unsigned hpk0, hpk1;
    { const int b = c >> 3, dq = (c >> 2) & 1; const int fl = rh * 4 * nch + 4 * (w + 4 * dq) + g;
      const bool act = (w + 4 * dq) < nch;
      const int go = WS_S1M + (h * 8 + 2 * (c & 3)) * 1024 + (b * 24 + (fl >> 1)) * 16;
      float gv, uv;
      _Pragma("unroll 1") for (unsigned n = 0; n < SPIN_CAP; ++n) {
        CBAR();
        const v4u pg = ldc(rw, go), pu = ldc(rw, go + 1024);
        const bool ok = !act || ((pg.y == tA) & (pg.w == tB) & (pu.y == tA) & (pu.w == tB));
        gv = __uint_as_float((g & 1) ? pg.z : pg.x); uv = __uint_as_float((g & 1) ? pu.z : pu.x);
        if (__all(ok)) break;
      }
      gv += dppf<XOR1>(gv); uv += dppf<XOR1>(uv); gv += dppf<XOR2>(gv); uv += dppf<XOR2>(uv);
      gv = rbf(gv); uv = rbf(uv);
      const float sg = rbf(1.0f / (1.0f + __builtin_amdgcn_exp2f(-gv * 1.4426950408889634f)));
      const float hv = act ? sg * uv : 0.f;
      const unsigned hp = cvtpk(hv, hv);
      hpk0 = (unsigned)__builtin_amdgcn_update_dpp((int)hp, (int)hp, 0x114, 0xf, 0xa, false);
      hpk1 = (unsigned)__builtin_amdgcn_update_dpp((int)hp, (int)hp, 0x104, 0xf, 0x5, false);
    }
    STAMP(4);
    f32x4 acc = {0.f, 0.f, 0.f, 0.f};
    acc = mfma(mask & hpk0, Dn[0], acc);
    acc = mfma(mask & hpk1, Dn[1], acc);
    {
      const int b = c >> 3, dim = 64 * rh + 16 * w + 4 * (c & 3) + g;
      const int go = WS_S2 + (h * 8 + b * 4) * 2048;
      wait_ticks(DELAY2);
      float ov[4], mv, lv;
      _Pragma("unroll 1") for (unsigned n = 0; n < SPIN_CAP; ++n) {
        CBAR();
        v4u p[4];
#pragma unroll
        for (int s = 0; s < 4; ++s) p[s] = ldc(rw, go + s * 2048 + (dim >> 1) * 16);
        const v4u pm = ldc(rw, go + (c & 3) * 2048 + 1024);
        bool ok = (pm.y == tA) & (pm.w == tB);
#pragma unroll
        for (int s = 0; s < 4; ++s) { ok &= (p[s].y == tA) & (p[s].w == tB); ov[s] = __uint_as_float((g & 1) ? p[s].z : p[s].x); }
        mv = __uint_as_float(pm.x); lv = __uint_as_float(pm.z);
        if (__all(ok)) break;
      }
      STAMP(5);
      float M = fmaxf(mv, dppf<XOR1>(mv)); M = fmaxf(M, dppf<XOR2>(M));
      const float e = __builtin_amdgcn_exp2f((mv - M) * 1.4426950408889634f);
      float Ls = e * lv; Ls += dppf<XOR1>(Ls); Ls += dppf<XOR2>(Ls);
      const float wt = e / Ls;
      float o = dppf<0x00>(wt) * ov[0];
      o = fmaf(dppf<0x55>(wt), ov[1], o); o = fmaf(dppf<0xAA>(wt), ov[2], o); o = fmaf(dppf<0xFF>(wt), ov[3], o);
      const unsigned opk = cvtpk(o, o);
      acc = mfma(mask & dppu<0x00>(opk), O[0], acc);
      acc = mfma(mask & dppu<0x55>(opk), O[1], acc);
      acc = mfma(mask & dppu<0xAA>(opk), O[2], acc);
      acc = mfma(mask & dppu<0xFF>(opk), O[3], acc);
    }
    *(LAS f32x4*)(L + L_RED2 + (w * 64 + lane) * 16) = acc;
    STAMP(6);
    if (lds_arrive(sync + C_2, lane) == 3u) {
      f32x4 t = *(LAS f32x4*)(L + L_RED2 + lane * 16);
#pragma unroll
      for (int k = 1; k < 4; ++k) t += *(LAS f32x4*)(L + L_RED2 + (k * 64 + lane) * 16);
      const int cg = (g >> 1) * 128 + 32 * cq + 2 * c + (g & 1);
      const int po = WS_S3 + cg * 2048 + (2 * h + rh) * 32;
      stc(rw, po, t.x, t.y, tA, tB); stc(rw, po + 16, t.z, t.w, tA, tB);
      CBAR();
      if (lane == 0) lds_st(sync + F_C, 1u);
      STAMP(7);
    }
  }
  STAMP_FLUSH();
}

extern "C" void kernel_launch(void* const* d_in, const int* in_sizes, int n_in, void* d_out, int out_size, void* d_ws, size_t ws_size, hipStream_t stream) {
  fused_decode<<<256, 1024, 0, stream>>>((const u16*)d_in[0], (const u16*)d_in[1], (const u16*)d_in[2], (const u16*)d_in[3], (const u16*)d_in[4],
                                         (const u16*)d_in[6], (const u16*)d_in[7], (const u16*)d_in[8], (u16*)d_out, (unsigned char*)d_ws);
}
```

```cpp
#define AMPWAVE 3
#define AMPSTAMP 0
#define AMPWG 77
#define AMPK 4
#define DELAY1 80
#define DELAYM 100
#define BCAP 10
#define PF_LINES 80
#include <hip/hip_runtime.h>
#include <stdint.h>

typedef unsigned short u16;
typedef unsigned v4u __attribute__((ext_vector_type(4)));
typedef unsigned v2u __attribute__((ext_vector_type(2)));
typedef float f32x4 __attribute__((ext_vector_type(4)));
typedef float f32x2 __attribute__((ext_vector_type(2)));
typedef short bf16x8 __attribute__((ext_vector_type(8)));
typedef __bf16 bf16x2 __attribute__((ext_vector_type(2)));
#define LAS __attribute__((address_space(3)))

#define DELAY2 30
#define DELAY3 30
#define CPRIO 1
#define DELAY1B 80
#define KFIRST 1
#define TLBWARM 0
#define TLBWAVE 15
#define GATEB2 170

namespace {
constexpr int AUX_NT = 2, AUX_SC1 = 16;
constexpr unsigned RSRC3 = 0x00020000u;
constexpr int WS_EPOCH = 0;
constexpr int WS_S1Q = 4096;
constexpr int WS_S1M = WS_S1Q + 256 * 1024;
constexpr int WS_S2 = WS_S1M + 256 * 1024;
constexpr int WS_S3 = WS_S2 + 256 * 2048;
constexpr int WS_END = WS_S3 + 256 * 2048;
constexpr int WS_STAMP = 8 << 20;
constexpr int L_SYNC = 0;
constexpr int L_STAT = 128;
constexpr int L_Q = 256;
constexpr int L_X = 512;
constexpr int L_RED1 = 2560;
constexpr int L_REDM = L_RED1 + 4096;
constexpr int L_RED2 = L_REDM + 3072;
constexpr int L_OB = L_RED2 + 4096;
constexpr int L_ML = L_OB + 4096;
constexpr int L_MX = L_SYNC + 96;
constexpr int L_H = L_ML + 64;
constexpr int L_QS = L_H + 2048;
constexpr int L_P = L_QS + 2048;
constexpr int L_END = L_P + 1024;
enum { C_A = 0, C_B = 1, C_M = 2, C_2 = 3, F_Q = 4, F_X = 5, F_C = 6, W_TAG = 7, C_MX = 8, F_B = 9, F_P = 10, C_Q = 11, F_H = 12  , F_PM = 16, F_PR = 17  , C_BI = 21, C_KI = 22 };
constexpr unsigned SPIN_CAP = 1u << 22;

__device__ __forceinline__ float bf2f(unsigned v) { return __uint_as_float(v << 16); }
__device__ __forceinline__ float bflo(unsigned v) { return __uint_as_float(v << 16); }
__device__ __forceinline__ float bfhi(unsigned v) { return __uint_as_float(v & 0xffff0000u); }
__device__ __forceinline__ unsigned cvtpk(float a, float b) { f32x2 v = {a, b}; return __builtin_bit_cast(unsigned, __builtin_convertvector(v, bf16x2)); }
__device__ __forceinline__ float rbf(float a) { return bflo(cvtpk(a, a)); }
template <int CTRL> __device__ __forceinline__ unsigned dppu(unsigned x) { return (unsigned)__builtin_amdgcn_mov_dpp((int)x, CTRL, 0xf, 0xf, true); }
template <int CTRL> __device__ __forceinline__ float dppf(float x) { return __uint_as_float(dppu<CTRL>(__float_as_uint(x))); }
constexpr int XOR1 = 0xB1, XOR2 = 0x4E, XOR7 = 0x141, MIR = 0x140, ROR4 = 0x124, ROR8 = 0x128, NEWB = 0x150;
__device__ __forceinline__ float fmx(float a, float b) { float r; asm("v_max_f32 %0, %1, %2" : "=v"(r) : "v"(a), "v"(b)); return r; }
#define MAXDPP(x, ctrl) do { asm("s_nop 1\n\tv_max_f32_dpp %0, %1, %1 " ctrl " row_mask:0xf bank_mask:0xf" : "=v"(x) : "0"(x)); } while (0)
__device__ __forceinline__ float x16_sum(float x) { auto s = __builtin_amdgcn_permlane16_swap(__float_as_uint(x), __float_as_uint(x), false, false); return __uint_as_float(s[0]) + __uint_as_float(s[1]); }
__device__ __forceinline__ float x32_sum(float x) { auto s = __builtin_amdgcn_permlane32_swap(__float_as_uint(x), __float_as_uint(x), false, false); return __uint_as_float(s[0]) + __uint_as_float(s[1]); }
__device__ __forceinline__ float x16_max(float x) { auto s = __builtin_amdgcn_permlane16_swap(__float_as_uint(x), __float_as_uint(x), false, false); return fmx(__uint_as_float(s[0]), __uint_as_float(s[1])); }
__device__ __forceinline__ float x32_max(float x) { auto s = __builtin_amdgcn_permlane32_swap(__float_as_uint(x), __float_as_uint(x), false, false); return fmx(__uint_as_float(s[0]), __uint_as_float(s[1])); }
__device__ __forceinline__ f32x4 mfma(v4u a, v4u b, f32x4 c) { return __builtin_amdgcn_mfma_f32_16x16x32_bf16(__builtin_bit_cast(bf16x8, a), __builtin_bit_cast(bf16x8, b), c, 0, 0, 0); }
__device__ __forceinline__ float dot2(unsigned a, unsigned b, float c) { return __builtin_amdgcn_fdot2_f32_bf16(__builtin_bit_cast(bf16x2, a), __builtin_bit_cast(bf16x2, b), c, false); }
__device__ __forceinline__ __amdgpu_buffer_rsrc_t rsrc(const void* p, unsigned bytes) { return __builtin_amdgcn_make_buffer_rsrc((void*)p, 0, (int)bytes, RSRC3); }
__device__ __forceinline__ v4u ldnt(__amdgpu_buffer_rsrc_t r, int voff, int soff) { return __builtin_amdgcn_raw_buffer_load_b128(r, voff, soff, AUX_NT); }
__device__ __forceinline__ v4u ldc(__amdgpu_buffer_rsrc_t r, int voff) { return __builtin_amdgcn_raw_buffer_load_b128(r, voff, 0, AUX_SC1); }
__device__ __forceinline__ void stc(__amdgpu_buffer_rsrc_t r, int voff, float a, float b, unsigned tA, unsigned tB) {
  v4u v = {__float_as_uint(a), tA, __float_as_uint(b), tB};
  __builtin_amdgcn_raw_buffer_store_b128(v, r, voff, 0, AUX_SC1);
}
__device__ __forceinline__ void stc_hidden(const unsigned char* ws, int voff, float a, float b, unsigned tA, unsigned tB) {
  v4u v = {__float_as_uint(a), tA, __float_as_uint(b), tB};
  asm volatile("global_store_dwordx4 %0, %1, %2 sc1\n\ts_nop 1" :: "v"(voff), "v"(v), "s"(ws) : "memory");
}
#define CBAR() asm volatile("" ::: "memory")
#define SB() __builtin_amdgcn_sched_barrier(0)
__device__ __forceinline__ unsigned long long rt() { return __builtin_amdgcn_s_memrealtime(); }
__device__ __forceinline__ void wait_ticks(int n) { if (n > 0) { const unsigned long long t = rt() + n; while (rt() < t) __builtin_amdgcn_s_sleep(1); } }
__device__ __forceinline__ unsigned lds_ld(LAS unsigned* p) { return __hip_atomic_load(p, __ATOMIC_RELAXED, __HIP_MEMORY_SCOPE_WORKGROUP); }
__device__ __forceinline__ void lds_st(LAS unsigned* p, unsigned v) { __hip_atomic_store(p, v, __ATOMIC_RELAXED, __HIP_MEMORY_SCOPE_WORKGROUP); }
__device__ __forceinline__ void lds_wait(LAS unsigned* p) {
  _Pragma("unroll 1") for (unsigned n = 0; n < SPIN_CAP; ++n) { CBAR(); if (lds_ld(p) != 0u) break; __builtin_amdgcn_s_sleep(1); }
  CBAR();
}
__device__ __forceinline__ void lds_wait_eq(LAS unsigned* p, unsigned v) {
  _Pragma("unroll 1") for (unsigned n = 0; n < SPIN_CAP; ++n) { CBAR(); if (lds_ld(p) == v) break; __builtin_amdgcn_s_sleep(1); }
  CBAR();
}
__device__ __forceinline__ float rcpn(float d) { const float r = __builtin_amdgcn_rcpf(d); return fmaf(fmaf(-d, r, 1.0f), r, r); }
__device__ __forceinline__ void lds_add(LAS unsigned* p, int lane) { if (lane == 0) asm volatile("ds_add_u32 %0, %1" :: "v"((unsigned)(size_t)p), "v"(1u) : "memory"); CBAR(); }
__device__ __forceinline__ unsigned lds_arrive(LAS unsigned* p, int lane) {
  CBAR();
  unsigned old = 0;
  if (lane == 0) asm volatile("ds_add_rtn_u32 %0, %1, %2\n\ts_waitcnt lgkmcnt(0)" : "=v"(old) : "v"((unsigned)(size_t)p), "v"(1u) : "memory");
  old = __builtin_amdgcn_readfirstlane(old);
  CBAR();
  return old;
}
__device__ __forceinline__ float divr(float x, float d, float r) { const float q = x * r; return fmaf(fmaf(-q, d, x), r, q); }
#define STAMP(k) do { SB(); ts[k] = rt(); SB(); } while (0)
#define STAMP_DECL unsigned long long ts[10] = {0, 0, 0, 0, 0, 0, 0, 0, 0, 0}
#define STAMP_FLUSH() do { if (blockIdx.x == AMPWG && wave == AMPWAVE) { const unsigned long long e_ = ts[0] + 1500 + AMPK * (ts[AMPSTAMP] - ts[0]); while (rt() < e_) __builtin_amdgcn_s_sleep(4); } } while (0)
}

__global__ void __launch_bounds__(1024, 4)
fused_decode(const u16* __restrict__ x, const u16* __restrict__ kc, const u16* __restrict__ vc, const u16* __restrict__ wq,
             const u16* __restrict__ wo, const u16* __restrict__ wg, const u16* __restrict__ wu, const u16* __restrict__ wd,
             u16* __restrict__ out, unsigned char* __restrict__ ws) {
  __shared__ __attribute__((aligned(16))) unsigned char smem[L_END];
  LAS unsigned char* L = (LAS unsigned char*)smem;
  LAS unsigned* sync = (LAS unsigned*)(L + L_SYNC);
  const int tid = threadIdx.x, lane = tid & 63, wave = __builtin_amdgcn_readfirstlane(tid >> 6);
  const int wgid = blockIdx.x, h = (wgid & 7) * 4 + (wgid >> 6), j = (wgid >> 3) & 7;
  STAMP_DECL;
  STAMP(0);
  unsigned pf = 0, pf2 = 0;
  if (wave == 15) {
    const unsigned long long pc = __builtin_amdgcn_s_getpc() & ~127ull;
    pf = *(const __attribute__((address_space(1))) unsigned*)(pc + lane * 128);
    if (PF_LINES > 64) pf2 = *(const __attribute__((address_space(1))) unsigned*)(pc + (64 + lane < PF_LINES ? 64 + lane : 0) * 128);
  }
  asm volatile("" :: "s"(x), "s"(kc), "s"(vc), "s"(wq), "s"(wo), "s"(wg), "s"(wu), "s"(wd), "s"(out), "s"(ws));
#define LANE_CONSTS() int ln_ = lane; asm volatile("" : "+v"(ln_)); const int g = ln_ >> 4, c = ln_ & 15; \
    \
  v4u mask; { const unsigned half = (c & 1) ? 0xffff0000u : 0x0000ffffu; const int kk = (c & 7) >> 1; \
    mask.x = kk == 0 ? half : 0u; mask.y = kk == 1 ? half : 0u; mask.z = kk == 2 ? half : 0u; mask.w = kk == 3 ? half : 0u; } \
  const __amdgpu_buffer_rsrc_t rw = rsrc(ws, WS_END)
#define MLP_GROUP() const int nch = 5 + ((0x13 >> (h & 7)) & 1), c0 = 5 * h + 3 * (h >> 3) + ((0x33322210 >> (4 * (h & 7))) & 7)

  if (wave >= 12) {
    const int w = wave - 12;
    __builtin_amdgcn_s_barrier();
    CBAR();
    MLP_GROUP();
    const int cq = j & 3, rh = j >> 2;
    const __amdgpu_buffer_rsrc_t ro = rsrc(wo, 4096 * 512 * 2), rd = rsrc(wd, 1376 * 512 * 2);
    v4u O[4], Dn[2];
    lds_wait_eq(sync + C_BI, 4u);
    { const int voff = (lane >> 4) * 1024 + (lane & 15) * 16, soff = (128 * h + 64 * rh + 16 * w) * 1024 + cq * 256;
      const int so2 = (8 * c0 + rh * 4 * nch + 4 * w) * 1024 + cq * 256;
      Dn[0] = ldnt(rd, voff, so2); SB();
      Dn[1] = ldnt(rd, (w + 4 < nch) ? voff : 0x7fffffff - (1 << 24), so2 + 16 * 1024); SB();
#pragma unroll
      for (int i = 0; i < 4; ++i) { O[i] = ldnt(ro, voff, soff + i * 4096); SB(); } }
    STAMP(1);
    LANE_CONSTS();
    lds_wait(sync + F_H + (w >> 1));
    const unsigned tA = lds_ld(sync + W_TAG), tB = ~tA;
    f32x4 acc = {0.f, 0.f, 0.f, 0.f};
    acc = mfma(mask & *(LAS unsigned*)(L + L_H + (w * 128 + lane) * 4), Dn[0], acc);
    acc = mfma(mask & *(LAS unsigned*)(L + L_H + (w * 128 + 64 + lane) * 4), Dn[1], acc);
    STAMP(5);
    {
      const int b = c >> 3, dim = 64 * rh + 16 * w + 4 * (c & 3) + g;
      const int go = WS_S2 + (h * 8 + b * 4) * 2048;
      lds_wait(sync + F_B);
      wait_ticks(DELAY2);
      float ov[4], mv, lv;
      _Pragma("unroll 1") for (unsigned n = 0; n < SPIN_CAP; ++n) {
        CBAR();
        v4u p[4];
#pragma unroll
        for (int s = 0; s < 4; ++s) p[s] = ldc(rw, go + s * 2048 + (dim >> 1) * 16);
        const v4u pm = ldc(rw, go + (c & 3) * 2048 + 1024);
        bool ok = (pm.y == tA) & (pm.w == tB);
#pragma unroll
        for (int s = 0; s < 4; ++s) { ok &= (p[s].y == tA) & (p[s].w == tB); ov[s] = __uint_as_float((g & 1) ? p[s].z : p[s].x); }
        mv = __uint_as_float(pm.x); lv = __uint_as_float(pm.z);
        if (__all(ok)) break;
      }
      STAMP(6);
      float M = mv; MAXDPP(M, "quad_perm:[1,0,3,2]"); MAXDPP(M, "quad_perm:[2,3,0,1]");
      const float e = __builtin_amdgcn_exp2f((mv - M) * 1.4426950408889634f);
      float Ls = e * lv; Ls += dppf<XOR1>(Ls); Ls += dppf<XOR2>(Ls);
      const float wt = e * __builtin_amdgcn_rcpf(Ls);
      float o = dppf<0x00>(wt) * ov[0];
      o = fmaf(dppf<0x55>(wt), ov[1], o); o = fmaf(dppf<0xAA>(wt), ov[2], o); o = fmaf(dppf<0xFF>(wt), ov[3], o);
      const unsigned opk = cvtpk(o, o);
      acc = mfma(mask & dppu<0x00>(opk), O[0], acc);
      acc = mfma(mask & dppu<0x55>(opk), O[1], acc);
      acc = mfma(mask & dppu<0xAA>(opk), O[2], acc);
      acc = mfma(mask & dppu<0xFF>(opk), O[3], acc);
    }
    *(LAS f32x4*)(L + L_RED2 + (w * 64 + lane) * 16) = acc;
    STAMP(7);
    if (lds_arrive(sync + C_2, lane) == 3u) {
      f32x4 t = *(LAS f32x4*)(L + L_RED2 + lane * 16);
#pragma unroll
      for (int k = 1; k < 4; ++k) t += *(LAS f32x4*)(L + L_RED2 + (k * 64 + lane) * 16);
      const int cg = (g >> 1) * 128 + 32 * cq + 2 * c + (g & 1);
      const int po = WS_S3 + cg * 2048 + (2 * h + rh) * 32;
      stc(rw, po, t.x, t.y, tA, tB); stc(rw, po + 16, t.z, t.w, tA, tB);
      CBAR();
      if (lane == 0) lds_st(sync + F_C, 1u);
      STAMP(8);
    }
    asm volatile("" :: "v"(pf), "v"(pf2));
    } else if (wave >= 4) {
    const int kw = (wave - 4) & 3;
    if (wave == 4) { if (lane < 32) sync[lane] = 0u; asm volatile("s_waitcnt lgkmcnt(0)" ::: "memory"); }
    __builtin_amdgcn_s_barrier();
    CBAR();
    const int soff = (((j >> 2) * 32 + h) * 4096 + 3072 + (j & 3) * 256 + kw * 64) * 256;
#define CAPWAIT() asm volatile("s_waitcnt vmcnt(%0)" :: "n"(BCAP - 1) : "memory")
    v4u T[16];
    if (wave < 8) {
      const __amdgpu_buffer_rsrc_t rk = rsrc(kc, 2u * 32 * 4096 * 128 * 2);
#pragma unroll
      for (int i = 0; i < 16; ++i) { if (i >= BCAP) CAPWAIT(); T[i] = ldnt(rk, lane * 16 + (i & 3) * 1024, soff + (i >> 2) * 4096); SB(); }
      lds_add(sync + C_KI, lane);
      STAMP(1);
      const int g = lane >> 4, c = lane & 15;
      lds_wait(sync + F_Q);
      STAMP(2);
      const v4u q = *(LAS v4u*)(L + L_Q + c * 16);
      float sc[16];
#pragma unroll
      for (int i = 0; i < 16; ++i) sc[i] = dot2(T[i].w, q.w, dot2(T[i].z, q.z, dot2(T[i].y, q.y, dot2(T[i].x, q.x, 0.f))));
      { const bool b3 = c & 8, b2 = c & 4, b1 = c & 2, b0 = c & 1;
#pragma unroll
        for (int i = 0; i < 8; ++i) { const float keep = b3 ? sc[i + 8] : sc[i], send = b3 ? sc[i] : sc[i + 8]; sc[i] = keep + dppf<ROR8>(send); }
#pragma unroll
        for (int i = 0; i < 4; ++i) { const float keep = b2 ? sc[i + 4] : sc[i], send = b2 ? sc[i] : sc[i + 4]; sc[i] = keep + dppf<XOR7>(send); }
#pragma unroll
        for (int i = 0; i < 2; ++i) { const float keep = b1 ? sc[i + 2] : sc[i], send = b1 ? sc[i] : sc[i + 2]; sc[i] = keep + dppf<XOR2>(send); }
        { const float keep = b0 ? sc[1] : sc[0], send = b0 ? sc[0] : sc[1]; sc[0] = keep + dppf<XOR1>(send); } }
      const float sv = rbf(rbf(sc[0]) * 0.08838834764831845f);
      float m = sv; MAXDPP(m, "quad_perm:[1,0,3,2]"); MAXDPP(m, "quad_perm:[2,3,0,1]"); MAXDPP(m, "row_half_mirror"); MAXDPP(m, "row_mirror"); m = x32_max(x16_max(m));
      if (lane == 0) lds_st((LAS unsigned*)(L + L_MX) + kw, __float_as_uint(m + 0.0f) ^ 0x80000000u);
      { unsigned mv;
        _Pragma("unroll 1") for (unsigned n = 0; n < SPIN_CAP; ++n) {
          CBAR();
          mv = lds_ld((LAS unsigned*)(L + L_MX) + (lane & 3));
          if (__all(mv != 0u)) break;
        }
        m = __uint_as_float(mv ^ 0x80000000u);
        MAXDPP(m, "quad_perm:[1,0,3,2]"); MAXDPP(m, "quad_perm:[2,3,0,1]"); }
      const float p = __builtin_amdgcn_exp2f((sv - m) * 1.4426950408889634f);
      *(LAS unsigned*)(L + L_P + ((kw * 4 + g) * 16 + c) * 4) = cvtpk(p, p);
      float l = p + dppf<XOR1>(p); l += dppf<XOR2>(l); l += dppf<XOR7>(l); l += dppf<MIR>(l); l = x32_sum(x16_sum(l));
      if (lane == 0) { *(LAS float*)(L + L_ML + kw * 4) = l; *(LAS float*)(L + L_ML + 16) = m; }
      CBAR();
      if (lane == 0) lds_st(sync + F_PR + kw, 1u);
      STAMP(3);
    } else {
      const __amdgpu_buffer_rsrc_t rv = rsrc(vc, 2u * 32 * 4096 * 128 * 2);
      lds_wait_eq(sync + C_KI, 4u);
#pragma unroll
      for (int i = 0; i < 16; ++i) { if (i >= BCAP) CAPWAIT(); T[i] = ldnt(rv, lane * 16 + (i & 3) * 1024, soff + (i >> 2) * 4096); SB(); }
      lds_add(sync + C_BI, lane);
      STAMP(1);
      LANE_CONSTS();
      lds_wait(sync + F_PR + kw);
      STAMP(2);
      v4u pp[4];
#pragma unroll
      for (int k = 0; k < 4; ++k) pp[k] = *(LAS v4u*)(L + L_P + (kw * 4 + g) * 64 + k * 16);
      const v4u mk = (c & 8) ? v4u{0u, 0u, 0u, 0u} : mask;
      f32x4 acc = {0.f, 0.f, 0.f, 0.f};
#pragma unroll
      for (int i = 0; i < 16; ++i) acc = mfma(mk & pp[i >> 2][i & 3], T[i], acc);
      if (lane < 32) *(LAS f32x4*)(L + L_OB + kw * 512 + (8 * c + 4 * g) * 4) = acc;
      STAMP(4);
      if (lds_arrive(sync + C_B, lane) == 3u) {
        const unsigned tA = lds_ld(sync + W_TAG), tB = ~tA;
        f32x2 o[4];
#pragma unroll
        for (int k = 0; k < 4; ++k) o[k] = *(LAS f32x2*)(L + L_OB + k * 512 + lane * 8);
        const f32x4 la = *(LAS f32x4*)(L + L_ML); const float m = *(LAS float*)(L + L_ML + 16);
        const f32x2 os = (o[0] + o[1]) + (o[2] + o[3]);
        const float Ls = (la.x + la.y) + (la.z + la.w);
        const int po = WS_S2 + (h * 8 + j) * 2048;
        stc(rw, po + lane * 16, os.x, os.y, tA, tB);
        if (lane == 0) { stc(rw, po + 1024, m, Ls, tA, tB); lds_st(sync + F_B, 1u); }
        STAMP(5);
      }
    }
  } else {
    const int a = wave;
    const __amdgpu_buffer_rsrc_t rx = rsrc(x, 2048);
    const v4u x0 = __builtin_amdgcn_raw_buffer_load_b128(rx, lane * 32, 0, 0), x1 = __builtin_amdgcn_raw_buffer_load_b128(rx, lane * 32 + 16, 0, 0);
    SB();
    const int rqt = j >> 1;
    v4u w[4], G[4];
    { const __amdgpu_buffer_rsrc_t rq = rsrc(wq, 512 * 4096 * 2);
      const int voff = (lane >> 3) * 8192 + (lane & 7) * 16, soff = (128 * rqt + 32 * a) * 8192 + h * 256 + (j & 1) * 128;
#pragma unroll
      for (int i = 0; i < 4; ++i) { w[i] = ldnt(rq, voff, soff + i * 8 * 8192); SB(); } }
    MLP_GROUP();
    const __amdgpu_buffer_rsrc_t rg = rsrc((j & 1) ? wu : wg, 512 * 1376 * 2);
    { const int p = lane & 7; const int voff = p < nch ? (lane >> 3) * 2752 + p * 16 : 0x7fffffff - (1 << 24);
      const int soff = (128 * rqt + 32 * a) * 2752 + c0 * 16;
#pragma unroll
      for (int i = 0; i < 4; ++i) { G[i] = ldnt(rg, voff, soff + i * 8 * 2752); SB(); } }
    unsigned dummy = __builtin_amdgcn_raw_buffer_load_b32(rg, 0x7fffffff - (1 << 24), 0, 0);
    SB();
    unsigned ep;
    asm volatile("s_load_dword %0, %1, 0x0" : "=s"(ep) : "s"(ws) : "memory");
    __builtin_amdgcn_s_barrier();
    CBAR();
    STAMP(1);
    LANE_CONSTS();
    f32x4 st;
    {
      *(LAS v4u*)(L + L_X + lane * 32) = x0; *(LAS v4u*)(L + L_X + lane * 32 + 16) = x1;
      float xv[16];
      { const unsigned r[8] = {x0.x, x0.y, x0.z, x0.w, x1.x, x1.y, x1.z, x1.w};
#pragma unroll
        for (int i = 0; i < 8; ++i) { xv[2 * i] = bflo(r[i]); xv[2 * i + 1] = bfhi(r[i]); } }
      float s = 0.f;
#pragma unroll
      for (int i = 0; i < 16; i += 4) s += (xv[i] + xv[i + 1]) + (xv[i + 2] + xv[i + 3]);
      s += dppf<XOR1>(s); s += dppf<XOR2>(s); s += dppf<XOR7>(s); s += dppf<MIR>(s); s = x16_sum(s);
      const float mean = s * (1.0f / 512.0f);
      float v0 = 0.f, v1 = 0.f;
#pragma unroll
      for (int i = 0; i < 16; i += 2) { const float d = xv[i] - mean, e = xv[i + 1] - mean; v0 = fmaf(d, d, v0); v1 = fmaf(e, e, v1); }
      float v = v0 + v1;
      v += dppf<XOR1>(v); v += dppf<XOR2>(v); v += dppf<XOR7>(v); v += dppf<MIR>(v); v = x16_sum(v);
      const float mb = rbf(mean), den = rbf(sqrtf(rbf(rbf(v * (1.0f / 512.0f)) + 1e-5f)));
      st.x = __uint_as_float(__builtin_amdgcn_readlane(__float_as_uint(mb), 0)); st.y = __uint_as_float(__builtin_amdgcn_readlane(__float_as_uint(den), 0));
      st.z = __uint_as_float(__builtin_amdgcn_readlane(__float_as_uint(mb), 32)); st.w = __uint_as_float(__builtin_amdgcn_readlane(__float_as_uint(den), 32));
    }
    asm volatile("s_waitcnt lgkmcnt(0)" : "+s"(ep) :: "memory");
    const unsigned tA = ep + 1u, tB = ~tA;
    if (a == 0 && lane == 0) lds_st(sync + W_TAG, tA);
    STAMP(2);
    unsigned n0[4], n1[4];
    { const float r0 = rcpn(st.y), r1 = rcpn(st.w);
#pragma unroll
      for (int i = 0; i < 4; ++i) {
        const int row = 128 * rqt + 32 * a + 8 * i + 2 * g + (c >> 3);
        const unsigned raw0 = *(LAS u16*)(L + L_X + row * 2), raw1 = *(LAS u16*)(L + L_X + (512 + row) * 2);
        const float f0 = rbf(bf2f(raw0) - st.x) * r0, f1 = rbf(bf2f(raw1) - st.z) * r1;
        n0[i] = cvtpk(f0, f0); n1[i] = cvtpk(f1, f1);
      } }
    const bool valid = (g >> 1) == (c >> 3);
    { f32x4 a0 = {0.f, 0.f, 0.f, 0.f}, a1 = {0.f, 0.f, 0.f, 0.f};
#pragma unroll
      for (int i = 0; i < 4; ++i) { a0 = mfma(mask & n0[i], w[i], a0); a1 = mfma(mask & n1[i], w[i], a1); }
      if (valid) { const int o = L_RED1 + (a * 2 + (c >> 3)) * 512 + (8 * (c & 7) + 4 * (g & 1)) * 4;
        *(LAS f32x4*)(L + o) = a0; *(LAS f32x4*)(L + o + 256) = a1; } }
    lds_add(sync + C_A, lane);
    STAMP(3);
    if (a == 3) {
      lds_wait_eq(sync + C_A, 4u);
      f32x2 t = *(LAS f32x2*)(L + L_RED1 + lane * 8);
#pragma unroll
      for (int k = 1; k < 8; ++k) t += *(LAS f32x2*)(L + L_RED1 + k * 512 + lane * 8);
      stc_hidden(ws, WS_S1Q + (h * 8 + j) * 1024 + lane * 16, t.x, t.y, tA, tB);
      CBAR();
      if (lane == 0) lds_st(sync + F_P, (unsigned)rt() | 1u);
    }
    STAMP(4);
    { f32x4 a0 = {0.f, 0.f, 0.f, 0.f}, a1 = {0.f, 0.f, 0.f, 0.f};
#pragma unroll
      for (int i = 0; i < 4; ++i) { a0 = mfma(mask & n0[i], G[i], a0); a1 = mfma(mask & n1[i], G[i], a1); }
      if (valid && (c & 7) < 6) {
        const int o = L_REDM + ((a * 2 + (c >> 3)) * 2 * 48 + 8 * (c & 7) + 4 * (g & 1)) * 4;
        *(LAS f32x4*)(L + o) = a0; *(LAS f32x4*)(L + o + 48 * 4) = a1;
      } }
    lds_add(sync + C_M, lane);
    STAMP(6);
    if (a == 3) {
      lds_wait_eq(sync + C_M, 4u);
      f32x2 u = {0.f, 0.f};
      const int lm = lane < 48 ? lane : 47;
#pragma unroll
      for (int k = 0; k < 8; ++k) u += *(LAS f32x2*)(L + L_REDM + k * 384 + lm * 8);
      if (lane < 48) stc_hidden(ws, WS_S1M + (h * 8 + j) * 1024 + lane * 16, u.x, u.y, tA, tB);
      CBAR();
      if (lane == 0) lds_st(sync + F_PM, (unsigned)rt() | 1u);
      lds_wait(sync + F_C);
      STAMP(7);
      wait_ticks(DELAY3);
      float y0, y1;
      _Pragma("unroll 1") for (unsigned n = 0; n < SPIN_CAP; ++n) {
        CBAR();
        const v4u p0 = ldc(rw, WS_S3 + wgid * 2048 + lane * 16), p1 = ldc(rw, WS_S3 + wgid * 2048 + 1024 + lane * 16);
        const bool ok = (p0.y == tA) & (p0.w == tB) & (p1.y == tA) & (p1.w == tB);
        y0 = __uint_as_float(p0.x) + __uint_as_float(p1.x); y1 = __uint_as_float(p0.z) + __uint_as_float(p1.z);
        if (__all(ok)) break;
      }
      y0 += dppf<XOR2>(y0); y1 += dppf<XOR2>(y1);
      y0 += dppf<ROR4>(y0); y1 += dppf<ROR4>(y1);
      y0 += dppf<ROR8>(y0); y1 += dppf<ROR8>(y1);
      y0 = x32_sum(x16_sum(y0)); y1 = x32_sum(x16_sum(y1));
      const unsigned xo = *(LAS unsigned*)(L + L_X + wgid * 8 + (lane & 1) * 4);
      if (lane < 2) ((unsigned*)out)[wgid * 2 + lane] = cvtpk(bflo(xo) + y0, bfhi(xo) + y1);
      if (wgid == 0 && lane == 0) __builtin_amdgcn_raw_buffer_store_b32(tA, rw, WS_EPOCH, 0, AUX_SC1);
      STAMP(8);
    } else if (a == 0) {
      lds_wait(sync + F_P);
      const unsigned tp = lds_ld(sync + F_P);
      while ((int)((unsigned)rt() - tp) < DELAY1) __builtin_amdgcn_s_sleep(1);
      const int goq = WS_S1Q + (h * 8 + (lane >> 5)) * 1024 + ((j >> 2) * 32 + (lane & 31)) * 16;
      CBAR();
      v4u p[4], p2[4];
#pragma unroll
      for (int k = 0; k < 4; ++k) p[k] = ldc(rw, goq + k * 2048);
      SB();
      while ((int)((unsigned)rt() - tp) < DELAY1 + DELAY1B) __builtin_amdgcn_s_sleep(1);
      CBAR();
#pragma unroll
      for (int k = 0; k < 4; ++k) p2[k] = ldc(rw, goq + k * 2048);
      SB();
#define QOK(p) __all((p[0].y == tA) & (p[0].w == tB) & (p[1].y == tA) & (p[1].w == tB) & (p[2].y == tA) & (p[2].w == tB) & (p[3].y == tA) & (p[3].w == tB))
      bool dn = QOK(p);
      if (!dn) {
#pragma unroll
        for (int k = 0; k < 4; ++k) p[k] = p2[k];
        dn = QOK(p);
      }
      _Pragma("unroll 1") for (unsigned n = 0; !dn && n < SPIN_CAP; ++n) {
        CBAR();
#pragma unroll
        for (int k = 0; k < 4; ++k) p[k] = ldc(rw, goq + k * 2048);
        dn = QOK(p);
      }
#undef QOK
      *(LAS unsigned*)(L + L_Q + lane * 4) = cvtpk((__uint_as_float(p[0].x) + __uint_as_float(p[1].x)) + (__uint_as_float(p[2].x) + __uint_as_float(p[3].x)),
                                                   (__uint_as_float(p[0].z) + __uint_as_float(p[1].z)) + (__uint_as_float(p[2].z) + __uint_as_float(p[3].z)));
      CBAR();
      if (lane == 0) lds_st(sync + F_Q, 1u);
      STAMP(5);
    } else {
      lds_wait(sync + F_PM);
      { const unsigned t0 = lds_ld(sync + F_PM);
        while ((int)((unsigned)rt() - t0) < DELAYM) __builtin_amdgcn_s_sleep(1); }
      const int rh = j >> 2, dq = (c >> 2) & 1;
      int goh[2]; bool act[2]; v4u pg[2], pu[2];
#pragma unroll
      for (int k = 0; k < 2; ++k) { const int cw = 2 * (a - 1) + k; const int fl = rh * 4 * nch + 4 * (cw + 4 * dq) + g;
        act[k] = (cw + 4 * dq) < nch;
        goh[k] = WS_S1M + (h * 8 + 2 * (c & 3)) * 1024 + ((c >> 3) * 24 + (fl >> 1)) * 16; }
      _Pragma("unroll 1") for (unsigned n = 0; n < SPIN_CAP; ++n) {
        CBAR();
        bool ok = true;
#pragma unroll
        for (int k = 0; k < 2; ++k) { pg[k] = ldc(rw, goh[k]); pu[k] = ldc(rw, goh[k] + 1024); }
#pragma unroll
        for (int k = 0; k < 2; ++k) ok &= !act[k] || ((pg[k].y == tA) & (pg[k].w == tB) & (pu[k].y == tA) & (pu[k].w == tB));
        if (__all(ok)) break;
      }
#pragma unroll
      for (int k = 0; k < 2; ++k) { const int cw = 2 * (a - 1) + k;
        float gv = __uint_as_float((g & 1) ? pg[k].z : pg[k].x), uv = __uint_as_float((g & 1) ? pu[k].z : pu[k].x);
        gv += dppf<XOR1>(gv); uv += dppf<XOR1>(uv); gv += dppf<XOR2>(gv); uv += dppf<XOR2>(uv);
        gv = rbf(gv); uv = rbf(uv);
        const float sg = rbf(__builtin_amdgcn_rcpf(1.0f + __builtin_amdgcn_exp2f(-gv * 1.4426950408889634f)));
        const float hv = act[k] ? sg * uv : 0.f;
        const unsigned hp = cvtpk(hv, hv);
        *(LAS unsigned*)(L + L_H + (cw * 128 + lane) * 4) = (unsigned)__builtin_amdgcn_update_dpp((int)hp, (int)hp, 0x114, 0xf, 0xa, false);
        *(LAS unsigned*)(L + L_H + (cw * 128 + 64 + lane) * 4) = (unsigned)__builtin_amdgcn_update_dpp((int)hp, (int)hp, 0x104, 0xf, 0x5, false);
      }
      CBAR();
      if (lane == 0) lds_st(sync + F_H + (a - 1), 1u);
      STAMP(5);
    }
    asm volatile("" :: "v"(dummy));
  }
  STAMP_FLUSH();
}

extern "C" void kernel_launch(void* const* d_in, const int* in_sizes, int n_in, void* d_out, int out_size, void* d_ws, size_t ws_size, hipStream_t stream) {
  fused_decode<<<256, 1024, 0, stream>>>((const u16*)d_in[0], (const u16*)d_in[1], (const u16*)d_in[2], (const u16*)d_in[3], (const u16*)d_in[4],
                                         (const u16*)d_in[6], (const u16*)d_in[7], (const u16*)d_in[8], (u16*)d_out, (unsigned char*)d_ws);
}
```

```cpp
#include <hip/hip_runtime.h>
#include <stdint.h>

typedef unsigned short u16;
typedef unsigned v4u __attribute__((ext_vector_type(4)));
typedef unsigned v2u __attribute__((ext_vector_type(2)));
typedef float f32x4 __attribute__((ext_vector_type(4)));
typedef float f32x2 __attribute__((ext_vector_type(2)));
typedef short bf16x8 __attribute__((ext_vector_type(8)));
typedef __bf16 bf16x2 __attribute__((ext_vector_type(2)));
#define LAS __attribute__((address_space(3)))

#define DELAY1 80
#define DELAYM 100
#define DELAY2 30
#define DELAY3 30
#define CPRIO 1
#define DELAY1B 80
#define KFIRST 1
#define TLBWARM 0
#define TLBWAVE 15
#define GATEB2 170
#define BCAP 9
#define PF_LINES 83

namespace {
constexpr int AUX_NT = 2, AUX_SC1 = 16;
constexpr unsigned RSRC3 = 0x00020000u;
constexpr int WS_EPOCH = 0;
constexpr int WS_S1Q = 4096;
constexpr int WS_S1M = WS_S1Q + 256 * 1024;
constexpr int WS_S2 = WS_S1M + 256 * 1024;
constexpr int WS_S3 = WS_S2 + 256 * 2048;
constexpr int WS_END = WS_S3 + 256 * 2048;
constexpr int WS_STAMP = 8 << 20;
constexpr int L_SYNC = 0;
constexpr int L_STAT = 128;
constexpr int L_Q = 256;
constexpr int L_X = 512;
constexpr int L_RED1 = 2560;
constexpr int L_REDM = L_RED1 + 4096;
constexpr int L_RED2 = L_REDM + 3072;
constexpr int L_OB = L_RED2 + 4096;
constexpr int L_ML = L_OB + 4096;
constexpr int L_MX = L_SYNC + 96;
constexpr int L_H = L_ML + 64;
constexpr int L_QS = L_H + 2048;
constexpr int L_P = L_QS + 2048;
constexpr int L_END = L_P + 1024;
enum { C_A = 0, C_B = 1, C_M = 2, C_2 = 3, F_Q = 4, F_X = 5, F_C = 6, W_TAG = 7, C_MX = 8, F_B = 9, F_P = 10, C_Q = 11, F_H = 12  , F_PM = 16, F_PR = 17  , C_BI = 21, C_KI = 22 };
constexpr unsigned SPIN_CAP = 1u << 22;

__device__ __forceinline__ float bf2f(unsigned v) { return __uint_as_float(v << 16); }
__device__ __forceinline__ float bflo(unsigned v) { return __uint_as_float(v << 16); }
__device__ __forceinline__ float bfhi(unsigned v) { return __uint_as_float(v & 0xffff0000u); }
__device__ __forceinline__ unsigned cvtpk(float a, float b) { f32x2 v = {a, b}; return __builtin_bit_cast(unsigned, __builtin_convertvector(v, bf16x2)); }
__device__ __forceinline__ float rbf(float a) { return bflo(cvtpk(a, a)); }
template <int CTRL> __device__ __forceinline__ unsigned dppu(unsigned x) { return (unsigned)__builtin_amdgcn_mov_dpp((int)x, CTRL, 0xf, 0xf, true); }
template <int CTRL> __device__ __forceinline__ float dppf(float x) { return __uint_as_float(dppu<CTRL>(__float_as_uint(x))); }
constexpr int XOR1 = 0xB1, XOR2 = 0x4E, XOR7 = 0x141, MIR = 0x140, ROR4 = 0x124, ROR8 = 0x128, NEWB = 0x150;
__device__ __forceinline__ float fmx(float a, float b) { float r; asm("v_max_f32 %0, %1, %2" : "=v"(r) : "v"(a), "v"(b)); return r; }
#define MAXDPP(x, ctrl) do { asm("s_nop 1\n\tv_max_f32_dpp %0, %1, %1 " ctrl " row_mask:0xf bank_mask:0xf" : "=v"(x) : "0"(x)); } while (0)
__device__ __forceinline__ float x16_sum(float x) { auto s = __builtin_amdgcn_permlane16_swap(__float_as_uint(x), __float_as_uint(x), false, false); return __uint_as_float(s[0]) + __uint_as_float(s[1]); }
__device__ __forceinline__ float x32_sum(float x) { auto s = __builtin_amdgcn_permlane32_swap(__float_as_uint(x), __float_as_uint(x), false, false); return __uint_as_float(s[0]) + __uint_as_float(s[1]); }
__device__ __forceinline__ float x16_max(float x) { auto s = __builtin_amdgcn_permlane16_swap(__float_as_uint(x), __float_as_uint(x), false, false); return fmx(__uint_as_float(s[0]), __uint_as_float(s[1])); }
__device__ __forceinline__ float x32_max(float x) { auto s = __builtin_amdgcn_permlane32_swap(__float_as_uint(x), __float_as_uint(x), false, false); return fmx(__uint_as_float(s[0]), __uint_as_float(s[1])); }
__device__ __forceinline__ f32x4 mfma(v4u a, v4u b, f32x4 c) { return __builtin_amdgcn_mfma_f32_16x16x32_bf16(__builtin_bit_cast(bf16x8, a), __builtin_bit_cast(bf16x8, b), c, 0, 0, 0); }
__device__ __forceinline__ float dot2(unsigned a, unsigned b, float c) { return __builtin_amdgcn_fdot2_f32_bf16(__builtin_bit_cast(bf16x2, a), __builtin_bit_cast(bf16x2, b), c, false); }
__device__ __forceinline__ __amdgpu_buffer_rsrc_t rsrc(const void* p, unsigned bytes) { return __builtin_amdgcn_make_buffer_rsrc((void*)p, 0, (int)bytes, RSRC3); }
__device__ __forceinline__ v4u ldnt(__amdgpu_buffer_rsrc_t r, int voff, int soff) { return __builtin_amdgcn_raw_buffer_load_b128(r, voff, soff, AUX_NT); }
__device__ __forceinline__ v4u ldc(__amdgpu_buffer_rsrc_t r, int voff) { return __builtin_amdgcn_raw_buffer_load_b128(r, voff, 0, AUX_SC1); }
__device__ __forceinline__ void stc(__amdgpu_buffer_rsrc_t r, int voff, float a, float b, unsigned tA, unsigned tB) {
  v4u v = {__float_as_uint(a), tA, __float_as_uint(b), tB};
  __builtin_amdgcn_raw_buffer_store_b128(v, r, voff, 0, AUX_SC1);
}
__device__ __forceinline__ void stc_hidden(const unsigned char* ws, int voff, float a, float b, unsigned tA, unsigned tB) {
  v4u v = {__float_as_uint(a), tA, __float_as_uint(b), tB};
  asm volatile("global_store_dwordx4 %0, %1, %2 sc1\n\ts_nop 1" :: "v"(voff), "v"(v), "s"(ws) : "memory");
}
#define CBAR() asm volatile("" ::: "memory")
#define SB() __builtin_amdgcn_sched_barrier(0)
__device__ __forceinline__ unsigned long long rt() { return __builtin_amdgcn_s_memrealtime(); }
__device__ __forceinline__ void wait_ticks(int n) { if (n > 0) { const unsigned long long t = rt() + n; while (rt() < t) __builtin_amdgcn_s_sleep(1); } }
__device__ __forceinline__ unsigned lds_ld(LAS unsigned* p) { return __hip_atomic_load(p, __ATOMIC_RELAXED, __HIP_MEMORY_SCOPE_WORKGROUP); }
__device__ __forceinline__ void lds_st(LAS unsigned* p, unsigned v) { __hip_atomic_store(p, v, __ATOMIC_RELAXED, __HIP_MEMORY_SCOPE_WORKGROUP); }
__device__ __forceinline__ void lds_wait(LAS unsigned* p) {
  _Pragma("unroll 1") for (unsigned n = 0; n < SPIN_CAP; ++n) { CBAR(); if (lds_ld(p) != 0u) break; __builtin_amdgcn_s_sleep(1); }
  CBAR();
}
__device__ __forceinline__ void lds_wait_eq(LAS unsigned* p, unsigned v) {
  _Pragma("unroll 1") for (unsigned n = 0; n < SPIN_CAP; ++n) { CBAR(); if (lds_ld(p) == v) break; __builtin_amdgcn_s_sleep(1); }
  CBAR();
}
__device__ __forceinline__ float rcpn(float d) { const float r = __builtin_amdgcn_rcpf(d); return fmaf(fmaf(-d, r, 1.0f), r, r); }
__device__ __forceinline__ void lds_add(LAS unsigned* p, int lane) { if (lane == 0) asm volatile("ds_add_u32 %0, %1" :: "v"((unsigned)(size_t)p), "v"(1u) : "memory"); CBAR(); }
__device__ __forceinline__ unsigned lds_arrive(LAS unsigned* p, int lane) {
  CBAR();
  unsigned old = 0;
  if (lane == 0) asm volatile("ds_add_rtn_u32 %0, %1, %2\n\ts_waitcnt lgkmcnt(0)" : "=v"(old) : "v"((unsigned)(size_t)p), "v"(1u) : "memory");
  old = __builtin_amdgcn_readfirstlane(old);
  CBAR();
  return old;
}
__device__ __forceinline__ float divr(float x, float d, float r) { const float q = x * r; return fmaf(fmaf(-q, d, x), r, q); }
#define STAMP(k) do {} while (0)
#define STAMP_DECL do {} while (0)
#define STAMP_FLUSH() do {} while (0)
}

__global__ void __launch_bounds__(1024, 4)
fused_decode(const u16* __restrict__ x, const u16* __restrict__ kc, const u16* __restrict__ vc, const u16* __restrict__ wq,
             const u16* __restrict__ wo, const u16* __restrict__ wg, const u16* __restrict__ wu, const u16* __restrict__ wd,
             u16* __restrict__ out, unsigned char* __restrict__ ws) {
  __shared__ __attribute__((aligned(16))) unsigned char smem[L_END];
  LAS unsigned char* L = (LAS unsigned char*)smem;
  LAS unsigned* sync = (LAS unsigned*)(L + L_SYNC);
  const int tid = threadIdx.x, lane = tid & 63, wave = __builtin_amdgcn_readfirstlane(tid >> 6);
  const int wgid = blockIdx.x, h = (wgid & 7) * 4 + (wgid >> 6), j = (wgid >> 3) & 7;
  STAMP_DECL;
  STAMP(0);
  unsigned pf = 0, pf2 = 0;
  if (wave == 15) {
    const unsigned long long pc = __builtin_amdgcn_s_getpc() & ~127ull;
    pf = *(const __attribute__((address_space(1))) unsigned*)(pc + lane * 128);
    if (PF_LINES > 64) pf2 = *(const __attribute__((address_space(1))) unsigned*)(pc + (64 + lane < PF_LINES ? 64 + lane : 0) * 128);
  }
  asm volatile("" :: "s"(x), "s"(kc), "s"(vc), "s"(wq), "s"(wo), "s"(wg), "s"(wu), "s"(wd), "s"(out), "s"(ws));
#define LANE_CONSTS() int ln_ = lane; asm volatile("" : "+v"(ln_)); const int g = ln_ >> 4, c = ln_ & 15; \
    \
  v4u mask; { const unsigned half = (c & 1) ? 0xffff0000u : 0x0000ffffu; const int kk = (c & 7) >> 1; \
    mask.x = kk == 0 ? half : 0u; mask.y = kk == 1 ? half : 0u; mask.z = kk == 2 ? half : 0u; mask.w = kk == 3 ? half : 0u; } \
  const __amdgpu_buffer_rsrc_t rw = rsrc(ws, WS_END)
#define MLP_GROUP() const int nch = 5 + ((0x13 >> (h & 7)) & 1), c0 = 5 * h + 3 * (h >> 3) + ((0x33322210 >> (4 * (h & 7))) & 7)

  if (wave >= 12) {
    const int w = wave - 12;
    __builtin_amdgcn_s_barrier();
    CBAR();
    MLP_GROUP();
    const int cq = j & 3, rh = j >> 2;
    const __amdgpu_buffer_rsrc_t ro = rsrc(wo, 4096 * 512 * 2), rd = rsrc(wd, 1376 * 512 * 2);
    v4u O[4], Dn[2];
    lds_wait_eq(sync + C_BI, 4u);
    { const int voff = (lane >> 4) * 1024 + (lane & 15) * 16, soff = (128 * h + 64 * rh + 16 * w) * 1024 + cq * 256;
      const int so2 = (8 * c0 + rh * 4 * nch + 4 * w) * 1024 + cq * 256;
      Dn[0] = ldnt(rd, voff, so2); SB();
      Dn[1] = ldnt(rd, (w + 4 < nch) ? voff : 0x7fffffff - (1 << 24), so2 + 16 * 1024); SB();
#pragma unroll
      for (int i = 0; i < 4; ++i) { O[i] = ldnt(ro, voff, soff + i * 4096); SB(); } }
    STAMP(1);
    LANE_CONSTS();
    lds_wait(sync + F_H + (w >> 1));
    const unsigned tA = lds_ld(sync + W_TAG), tB = ~tA;
    f32x4 acc = {0.f, 0.f, 0.f, 0.f};
    acc = mfma(mask & *(LAS unsigned*)(L + L_H + (w * 128 + lane) * 4), Dn[0], acc);
    acc = mfma(mask & *(LAS unsigned*)(L + L_H + (w * 128 + 64 + lane) * 4), Dn[1], acc);
    STAMP(5);
    {
      const int b = c >> 3, dim = 64 * rh + 16 * w + 4 * (c & 3) + g;
      const int go = WS_S2 + (h * 8 + b * 4) * 2048;
      lds_wait(sync + F_B);
      wait_ticks(DELAY2);
      float ov[4], mv, lv;
      _Pragma("unroll 1") for (unsigned n = 0; n < SPIN_CAP; ++n) {
        CBAR();
        v4u p[4];
#pragma unroll
        for (int s = 0; s < 4; ++s) p[s] = ldc(rw, go + s * 2048 + (dim >> 1) * 16);
        const v4u pm = ldc(rw, go + (c & 3) * 2048 + 1024);
        bool ok = (pm.y == tA) & (pm.w == tB);
#pragma unroll
        for (int s = 0; s < 4; ++s) { ok &= (p[s].y == tA) & (p[s].w == tB); ov[s] = __uint_as_float((g & 1) ? p[s].z : p[s].x); }
        mv = __uint_as_float(pm.x); lv = __uint_as_float(pm.z);
        if (__all(ok)) break;
      }
      STAMP(6);
      float M = mv; MAXDPP(M, "quad_perm:[1,0,3,2]"); MAXDPP(M, "quad_perm:[2,3,0,1]");
      const float e = __builtin_amdgcn_exp2f((mv - M) * 1.4426950408889634f);
      float Ls = e * lv; Ls += dppf<XOR1>(Ls); Ls += dppf<XOR2>(Ls);
      const float wt = e * __builtin_amdgcn_rcpf(Ls);
      float o = dppf<0x00>(wt) * ov[0];
      o = fmaf(dppf<0x55>(wt), ov[1], o); o = fmaf(dppf<0xAA>(wt), ov[2], o); o = fmaf(dppf<0xFF>(wt), ov[3], o);
      const unsigned opk = cvtpk(o, o);
      acc = mfma(mask & dppu<0x00>(opk), O[0], acc);
      acc = mfma(mask & dppu<0x55>(opk), O[1], acc);
      acc = mfma(mask & dppu<0xAA>(opk), O[2], acc);
      acc = mfma(mask & dppu<0xFF>(opk), O[3], acc);
    }
    *(LAS f32x4*)(L + L_RED2 + (w * 64 + lane) * 16) = acc;
    STAMP(7);
    if (lds_arrive(sync + C_2, lane) == 3u) {
      f32x4 t = *(LAS f32x4*)(L + L_RED2 + lane * 16);
#pragma unroll
      for (int k = 1; k < 4; ++k) t += *(LAS f32x4*)(L + L_RED2 + (k * 64 + lane) * 16);
      const int cg = (g >> 1) * 128 + 32 * cq + 2 * c + (g & 1);
      const int po = WS_S3 + cg * 2048 + (2 * h + rh) * 32;
      stc(rw, po, t.x, t.y, tA, tB); stc(rw, po + 16, t.z, t.w, tA, tB);
      CBAR();
      if (lane == 0) lds_st(sync + F_C, 1u);
      STAMP(8);
    }
    asm volatile("" :: "v"(pf), "v"(pf2));
    } else if (wave >= 4) {
    const int kw = (wave - 4) & 3;
    if (wave == 4) { if (lane < 32) sync[lane] = 0u; asm volatile("s_waitcnt lgkmcnt(0)" ::: "memory"); }
    __builtin_amdgcn_s_barrier();
    CBAR();
    const int soff = (((j >> 2) * 32 + h) * 4096 + 3072 + (j & 3) * 256 + kw * 64) * 256;
#define CAPWAIT() asm volatile("s_waitcnt vmcnt(%0)" :: "n"(BCAP - 1) : "memory")
    v4u T[16];
    if (wave < 8) {
      const __amdgpu_buffer_rsrc_t rk = rsrc(kc, 2u * 32 * 4096 * 128 * 2);
#pragma unroll
      for (int i = 0; i < 16; ++i) { if (i >= BCAP) CAPWAIT(); T[i] = ldnt(rk, lane * 16 + (i & 3) * 1024, soff + (i >> 2) * 4096); SB(); }
      lds_add(sync + C_KI, lane);
      STAMP(1);
      const int g = lane >> 4, c = lane & 15;
      lds_wait(sync + F_Q);
      STAMP(2);
      const v4u q = *(LAS v4u*)(L + L_Q + c * 16);
      float sc[16];
#pragma unroll
      for (int i = 0; i < 16; ++i) sc[i] = dot2(T[i].w, q.w, dot2(T[i].z, q.z, dot2(T[i].y, q.y, dot2(T[i].x, q.x, 0.f))));
      { const bool b3 = c & 8, b2 = c & 4, b1 = c & 2, b0 = c & 1;
#pragma unroll
        for (int i = 0; i < 8; ++i) { const float keep = b3 ? sc[i + 8] : sc[i], send = b3 ? sc[i] : sc[i + 8]; sc[i] = keep + dppf<ROR8>(send); }
#pragma unroll
        for (int i = 0; i < 4; ++i) { const float keep = b2 ? sc[i + 4] : sc[i], send = b2 ? sc[i] : sc[i + 4]; sc[i] = keep + dppf<XOR7>(send); }
#pragma unroll
        for (int i = 0; i < 2; ++i) { const float keep = b1 ? sc[i + 2] : sc[i], send = b1 ? sc[i] : sc[i + 2]; sc[i] = keep + dppf<XOR2>(send); }
        { const float keep = b0 ? sc[1] : sc[0], send = b0 ? sc[0] : sc[1]; sc[0] = keep + dppf<XOR1>(send); } }
      const float sv = rbf(rbf(sc[0]) * 0.08838834764831845f);
      float m = sv; MAXDPP(m, "quad_perm:[1,0,3,2]"); MAXDPP(m, "quad_perm:[2,3,0,1]"); MAXDPP(m, "row_half_mirror"); MAXDPP(m, "row_mirror"); m = x32_max(x16_max(m));
      if (lane == 0) lds_st((LAS unsigned*)(L + L_MX) + kw, __float_as_uint(m + 0.0f) ^ 0x80000000u);
      { unsigned mv;
        _Pragma("unroll 1") for (unsigned n = 0; n < SPIN_CAP; ++n) {
          CBAR();
          mv = lds_ld((LAS unsigned*)(L + L_MX) + (lane & 3));
          if (__all(mv != 0u)) break;
        }
        m = __uint_as_float(mv ^ 0x80000000u);
        MAXDPP(m, "quad_perm:[1,0,3,2]"); MAXDPP(m, "quad_perm:[2,3,0,1]"); }
      const float p = __builtin_amdgcn_exp2f((sv - m) * 1.4426950408889634f);
      *(LAS unsigned*)(L + L_P + ((kw * 4 + g) * 16 + c) * 4) = cvtpk(p, p);
      float l = p + dppf<XOR1>(p); l += dppf<XOR2>(l); l += dppf<XOR7>(l); l += dppf<MIR>(l); l = x32_sum(x16_sum(l));
      if (lane == 0) { *(LAS float*)(L + L_ML + kw * 4) = l; *(LAS float*)(L + L_ML + 16) = m; }
      CBAR();
      if (lane == 0) lds_st(sync + F_PR + kw, 1u);
      STAMP(3);
    } else {
      const __amdgpu_buffer_rsrc_t rv = rsrc(vc, 2u * 32 * 4096 * 128 * 2);
      lds_wait_eq(sync + C_KI, 4u);
#pragma unroll
      for (int i = 0; i < 16; ++i) { if (i >= BCAP) CAPWAIT(); T[i] = ldnt(rv, lane * 16 + (i & 3) * 1024, soff + (i >> 2) * 4096); SB(); }
      lds_add(sync + C_BI, lane);
      STAMP(1);
      LANE_CONSTS();
      lds_wait(sync + F_PR + kw);
      STAMP(2);
      v4u pp[4];
#pragma unroll
      for (int k = 0; k < 4; ++k) pp[k] = *(LAS v4u*)(L + L_P + (kw * 4 + g) * 64 + k * 16);
      const v4u mk = (c & 8) ? v4u{0u, 0u, 0u, 0u} : mask;
      f32x4 acc = {0.f, 0.f, 0.f, 0.f};
#pragma unroll
      for (int i = 0; i < 16; ++i) acc = mfma(mk & pp[i >> 2][i & 3], T[i], acc);
      if (lane < 32) *(LAS f32x4*)(L + L_OB + kw * 512 + (8 * c + 4 * g) * 4) = acc;
      STAMP(4);
      if (lds_arrive(sync + C_B, lane) == 3u) {
        const unsigned tA = lds_ld(sync + W_TAG), tB = ~tA;
        f32x2 o[4];
#pragma unroll
        for (int k = 0; k < 4; ++k) o[k] = *(LAS f32x2*)(L + L_OB + k * 512 + lane * 8);
        const f32x4 la = *(LAS f32x4*)(L + L_ML); const float m = *(LAS float*)(L + L_ML + 16);
        const f32x2 os = (o[0] + o[1]) + (o[2] + o[3]);
        const float Ls = (la.x + la.y) + (la.z + la.w);
        const int po = WS_S2 + (h * 8 + j) * 2048;
        stc(rw, po + lane * 16, os.x, os.y, tA, tB);
        if (lane == 0) { stc(rw, po + 1024, m, Ls, tA, tB); lds_st(sync + F_B, 1u); }
        STAMP(5);
      }
    }
  } else {
    const int a = wave;
    const __amdgpu_buffer_rsrc_t rx = rsrc(x, 2048);
    const v4u x0 = __builtin_amdgcn_raw_buffer_load_b128(rx, lane * 32, 0, 0), x1 = __builtin_amdgcn_raw_buffer_load_b128(rx, lane * 32 + 16, 0, 0);
    SB();
    const int rqt = j >> 1;
    v4u w[4], G[4];
    { const __amdgpu_buffer_rsrc_t rq = rsrc(wq, 512 * 4096 * 2);
      const int voff = (lane >> 3) * 8192 + (lane & 7) * 16, soff = (128 * rqt + 32 * a) * 8192 + h * 256 + (j & 1) * 128;
#pragma unroll
      for (int i = 0; i < 4; ++i) { w[i] = ldnt(rq, voff, soff + i * 8 * 8192); SB(); } }
    MLP_GROUP();
    const __amdgpu_buffer_rsrc_t rg = rsrc((j & 1) ? wu : wg, 512 * 1376 * 2);
    { const int p = lane & 7; const int voff = p < nch ? (lane >> 3) * 2752 + p * 16 : 0x7fffffff - (1 << 24);
      const int soff = (128 * rqt + 32 * a) * 2752 + c0 * 16;
#pragma unroll
      for (int i = 0; i < 4; ++i) { G[i] = ldnt(rg, voff, soff + i * 8 * 2752); SB(); } }
    unsigned dummy = __builtin_amdgcn_raw_buffer_load_b32(rg, 0x7fffffff - (1 << 24), 0, 0);
    SB();
    unsigned ep;
    asm volatile("s_load_dword %0, %1, 0x0" : "=s"(ep) : "s"(ws) : "memory");
    __builtin_amdgcn_s_barrier();
    CBAR();
    STAMP(1);
    LANE_CONSTS();
    f32x4 st;
    {
      *(LAS v4u*)(L + L_X + lane * 32) = x0; *(LAS v4u*)(L + L_X + lane * 32 + 16) = x1;
      float xv[16];
      { const unsigned r[8] = {x0.x, x0.y, x0.z, x0.w, x1.x, x1.y, x1.z, x1.w};
#pragma unroll
        for (int i = 0; i < 8; ++i) { xv[2 * i] = bflo(r[i]); xv[2 * i + 1] = bfhi(r[i]); } }
      float s = 0.f;
#pragma unroll
      for (int i = 0; i < 16; i += 4) s += (xv[i] + xv[i + 1]) + (xv[i + 2] + xv[i + 3]);
      s += dppf<XOR1>(s); s += dppf<XOR2>(s); s += dppf<XOR7>(s); s += dppf<MIR>(s); s = x16_sum(s);
      const float mean = s * (1.0f / 512.0f);
      float v0 = 0.f, v1 = 0.f;
#pragma unroll
      for (int i = 0; i < 16; i += 2) { const float d = xv[i] - mean, e = xv[i + 1] - mean; v0 = fmaf(d, d, v0); v1 = fmaf(e, e, v1); }
      float v = v0 + v1;
      v += dppf<XOR1>(v); v += dppf<XOR2>(v); v += dppf<XOR7>(v); v += dppf<MIR>(v); v = x16_sum(v);
      const float mb = rbf(mean), den = rbf(sqrtf(rbf(rbf(v * (1.0f / 512.0f)) + 1e-5f)));
      st.x = __uint_as_float(__builtin_amdgcn_readlane(__float_as_uint(mb), 0)); st.y = __uint_as_float(__builtin_amdgcn_readlane(__float_as_uint(den), 0));
      st.z = __uint_as_float(__builtin_amdgcn_readlane(__float_as_uint(mb), 32)); st.w = __uint_as_float(__builtin_amdgcn_readlane(__float_as_uint(den), 32));
    }
    asm volatile("s_waitcnt lgkmcnt(0)" : "+s"(ep) :: "memory");
    const unsigned tA = ep + 1u, tB = ~tA;
    if (a == 0 && lane == 0) lds_st(sync + W_TAG, tA);
    STAMP(2);
    unsigned n0[4], n1[4];
    { const float r0 = rcpn(st.y), r1 = rcpn(st.w);
#pragma unroll
      for (int i = 0; i < 4; ++i) {
        const int row = 128 * rqt + 32 * a + 8 * i + 2 * g + (c >> 3);
        const unsigned raw0 = *(LAS u16*)(L + L_X + row * 2), raw1 = *(LAS u16*)(L + L_X + (512 + row) * 2);
        const float f0 = rbf(bf2f(raw0) - st.x) * r0, f1 = rbf(bf2f(raw1) - st.z) * r1;
        n0[i] = cvtpk(f0, f0); n1[i] = cvtpk(f1, f1);
      } }
    const bool valid = (g >> 1) == (c >> 3);
    { f32x4 a0 = {0.f, 0.f, 0.f, 0.f}, a1 = {0.f, 0.f, 0.f, 0.f};
#pragma unroll
      for (int i = 0; i < 4; ++i) { a0 = mfma(mask & n0[i], w[i], a0); a1 = mfma(mask & n1[i], w[i], a1); }
      if (valid) { const int o = L_RED1 + (a * 2 + (c >> 3)) * 512 + (8 * (c & 7) + 4 * (g & 1)) * 4;
        *(LAS f32x4*)(L + o) = a0; *(LAS f32x4*)(L + o + 256) = a1; } }
    lds_add(sync + C_A, lane);
    STAMP(3);
    if (a == 3) {
      lds_wait_eq(sync + C_A, 4u);
      f32x2 t = *(LAS f32x2*)(L + L_RED1 + lane * 8);
#pragma unroll
      for (int k = 1; k < 8; ++k) t += *(LAS f32x2*)(L + L_RED1 + k * 512 + lane * 8);
      stc_hidden(ws, WS_S1Q + (h * 8 + j) * 1024 + lane * 16, t.x, t.y, tA, tB);
      CBAR();
      if (lane == 0) lds_st(sync + F_P, (unsigned)rt() | 1u);
    }
    STAMP(4);
    { f32x4 a0 = {0.f, 0.f, 0.f, 0.f}, a1 = {0.f, 0.f, 0.f, 0.f};
#pragma unroll
      for (int i = 0; i < 4; ++i) { a0 = mfma(mask & n0[i], G[i], a0); a1 = mfma(mask & n1[i], G[i], a1); }
      if (valid && (c & 7) < 6) {
        const int o = L_REDM + ((a * 2 + (c >> 3)) * 2 * 48 + 8 * (c & 7) + 4 * (g & 1)) * 4;
        *(LAS f32x4*)(L + o) = a0; *(LAS f32x4*)(L + o + 48 * 4) = a1;
      } }
    lds_add(sync + C_M, lane);
    STAMP(6);
    if (a == 3) {
      lds_wait_eq(sync + C_M, 4u);
      f32x2 u = {0.f, 0.f};
      const int lm = lane < 48 ? lane : 47;
#pragma unroll
      for (int k = 0; k < 8; ++k) u += *(LAS f32x2*)(L + L_REDM + k * 384 + lm * 8);
      if (lane < 48) stc_hidden(ws, WS_S1M + (h * 8 + j) * 1024 + lane * 16, u.x, u.y, tA, tB);
      CBAR();
      if (lane == 0) lds_st(sync + F_PM, (unsigned)rt() | 1u);
      lds_wait(sync + F_C);
      STAMP(7);
      wait_ticks(DELAY3);
      float y0, y1;
      _Pragma("unroll 1") for (unsigned n = 0; n < SPIN_CAP; ++n) {
        CBAR();
        const v4u p0 = ldc(rw, WS_S3 + wgid * 2048 + lane * 16), p1 = ldc(rw, WS_S3 + wgid * 2048 + 1024 + lane * 16);
        const bool ok = (p0.y == tA) & (p0.w == tB) & (p1.y == tA) & (p1.w == tB);
        y0 = __uint_as_float(p0.x) + __uint_as_float(p1.x); y1 = __uint_as_float(p0.z) + __uint_as_float(p1.z);
        if (__all(ok)) break;
      }
      y0 += dppf<XOR2>(y0); y1 += dppf<XOR2>(y1);
      y0 += dppf<ROR4>(y0); y1 += dppf<ROR4>(y1);
      y0 += dppf<ROR8>(y0); y1 += dppf<ROR8>(y1);
      y0 = x32_sum(x16_sum(y0)); y1 = x32_sum(x16_sum(y1));
      const unsigned xo = *(LAS unsigned*)(L + L_X + wgid * 8 + (lane & 1) * 4);
      if (lane < 2) ((unsigned*)out)[wgid * 2 + lane] = cvtpk(bflo(xo) + y0, bfhi(xo) + y1);
      if (wgid == 0 && lane == 0) __builtin_amdgcn_raw_buffer_store_b32(tA, rw, WS_EPOCH, 0, AUX_SC1);
      STAMP(8);
    } else if (a == 0) {
      lds_wait(sync + F_P);
      const unsigned tp = lds_ld(sync + F_P);
      while ((int)((unsigned)rt() - tp) < DELAY1) __builtin_amdgcn_s_sleep(1);
      const int goq = WS_S1Q + (h * 8 + (lane >> 5)) * 1024 + ((j >> 2) * 32 + (lane & 31)) * 16;
      CBAR();
      v4u p[4], p2[4];
#pragma unroll
      for (int k = 0; k < 4; ++k) p[k] = ldc(rw, goq + k * 2048);
      SB();
      while ((int)((unsigned)rt() - tp) < DELAY1 + DELAY1B) __builtin_amdgcn_s_sleep(1);
      CBAR();
#pragma unroll
      for (int k = 0; k < 4; ++k) p2[k] = ldc(rw, goq + k * 2048);
      SB();
#define QOK(p) __all((p[0].y == tA) & (p[0].w == tB) & (p[1].y == tA) & (p[1].w == tB) & (p[2].y == tA) & (p[2].w == tB) & (p[3].y == tA) & (p[3].w == tB))
      bool dn = QOK(p);
      if (!dn) {
#pragma unroll
        for (int k = 0; k < 4; ++k) p[k] = p2[k];
        dn = QOK(p);
      }
      _Pragma("unroll 1") for (unsigned n = 0; !dn && n < SPIN_CAP; ++n) {
        CBAR();
#pragma unroll
        for (int k = 0; k < 4; ++k) p[k] = ldc(rw, goq + k * 2048);
        dn = QOK(p);
      }
#undef QOK
      *(LAS unsigned*)(L + L_Q + lane * 4) = cvtpk((__uint_as_float(p[0].x) + __uint_as_float(p[1].x)) + (__uint_as_float(p[2].x) + __uint_as_float(p[3].x)),
                                                   (__uint_as_float(p[0].z) + __uint_as_float(p[1].z)) + (__uint_as_float(p[2].z) + __uint_as_float(p[3].z)));
      CBAR();
      if (lane == 0) lds_st(sync + F_Q, 1u);
      STAMP(5);
    } else {
      lds_wait(sync + F_PM);
      { const unsigned t0 = lds_ld(sync + F_PM);
        while ((int)((unsigned)rt() - t0) < DELAYM) __builtin_amdgcn_s_sleep(1); }
      const int rh = j >> 2, dq = (c >> 2) & 1;
      int goh[2]; bool act[2]; v4u pg[2], pu[2];
#pragma unroll
      for (int k = 0; k < 2; ++k) { const int cw = 2 * (a - 1) + k; const int fl = rh * 4 * nch + 4 * (cw + 4 * dq) + g;
        act[k] = (cw + 4 * dq) < nch;
        goh[k] = WS_S1M + (h * 8 + 2 * (c & 3)) * 1024 + ((c >> 3) * 24 + (fl >> 1)) * 16; }
      _Pragma("unroll 1") for (unsigned n = 0; n < SPIN_CAP; ++n) {
        CBAR();
        bool ok = true;
#pragma unroll
        for (int k = 0; k < 2; ++k) { pg[k] = ldc(rw, goh[k]); pu[k] = ldc(rw, goh[k] + 1024); }
#pragma unroll
        for (int k = 0; k < 2; ++k) ok &= !act[k] || ((pg[k].y == tA) & (pg[k].w == tB) & (pu[k].y == tA) & (pu[k].w == tB));
        if (__all(ok)) break;
      }
#pragma unroll
      for (int k = 0; k < 2; ++k) { const int cw = 2 * (a - 1) + k;
        float gv = __uint_as_float((g & 1) ? pg[k].z : pg[k].x), uv = __uint_as_float((g & 1) ? pu[k].z : pu[k].x);
        gv += dppf<XOR1>(gv); uv += dppf<XOR1>(uv); gv += dppf<XOR2>(gv); uv += dppf<XOR2>(uv);
        gv = rbf(gv); uv = rbf(uv);
        const float sg = rbf(__builtin_amdgcn_rcpf(1.0f + __builtin_amdgcn_exp2f(-gv * 1.4426950408889634f)));
        const float hv = act[k] ? sg * uv : 0.f;
        const unsigned hp = cvtpk(hv, hv);
        *(LAS unsigned*)(L + L_H + (cw * 128 + lane) * 4) = (unsigned)__builtin_amdgcn_update_dpp((int)hp, (int)hp, 0x114, 0xf, 0xa, false);
        *(LAS unsigned*)(L + L_H + (cw * 128 + 64 + lane) * 4) = (unsigned)__builtin_amdgcn_update_dpp((int)hp, (int)hp, 0x104, 0xf, 0x5, false);
      }
      CBAR();
      if (lane == 0) lds_st(sync + F_H + (a - 1), 1u);
      STAMP(5);
    }
    asm volatile("" :: "v"(dummy));
  }
  STAMP_FLUSH();
}

extern "C" void kernel_launch(void* const* d_in, const int* in_sizes, int n_in, void* d_out, int out_size, void* d_ws, size_t ws_size, hipStream_t stream) {
  fused_decode<<<256, 1024, 0, stream>>>((const u16*)d_in[0], (const u16*)d_in[1], (const u16*)d_in[2], (const u16*)d_in[3], (const u16*)d_in[4],
                                         (const u16*)d_in[6], (const u16*)d_in[7], (const u16*)d_in[8], (u16*)d_out, (unsigned char*)d_ws);
}
```
